```python
import torch

def _softmax(x, axis=-1):
    return torch.softmax(x, dim=axis)
def _gelu(x):
    return torch.nn.functional.gelu(x, approximate="tanh")
def _silu(x):
    return torch.nn.functional.silu(x)
def _sigmoid(x):
    return torch.sigmoid(x)
def _layernorm(x, axis=-1, eps=1e-5):
    m = x.mean(dim=axis, keepdim=True)
    v = x.var(dim=axis, keepdim=True, unbiased=False)
    return (x - m) / torch.sqrt(v + eps)
def _scatter_add(tgt, idx, src, axis=0):
    return tgt.clone().index_add_(axis, idx, src)
def _segment_sum(src, seg, n):
    out = torch.zeros((n,) + src.shape[1:], dtype=src.dtype)
    return out.index_add_(0, seg, src)
def _pad(x, pw):
    flat = tuple(v for pair in reversed(pw) for v in pair)
    return torch.nn.functional.pad(x, flat)

kernel_name = 'parallel_sum__gqa_block_cached__swiglu_ffn__decode'

def setup_inputs():

    _w = (lambda a: (lambda t: t.to(torch.bfloat16) if t.is_floating_point() else t)(torch.as_tensor(a)))
    def _A_setup():
        B, Sq, D = 1, 1, 576
        Hq, Hkv, Sk, Dh = 32, 16, 8192, 128
        assert Hq % Hkv == 0, (Hq, Hkv)
        return {
            "x": _w((lambda *s: torch.randn(*s, dtype=torch.bfloat16) * 0.05)(B, Sq, D)),
            "k_cache": _w((lambda *s: torch.randn(*s, dtype=torch.bfloat16) * 3.2)(B, Hkv, Sk, Dh)),
            "v_cache": _w((lambda *s: torch.randn(*s, dtype=torch.bfloat16) * 1.6)(B, Hkv, Sk, Dh)),
            "wq": _w((lambda *s: torch.randn(*s, dtype=torch.bfloat16) * 3.2)(D, Hq * Dh) * (D ** -0.5)),
            "wo": _w((lambda *s: torch.randn(*s, dtype=torch.bfloat16) * 1.6)(Hq * Dh, D) * ((Hq * Dh) ** -0.5)),
        }
    _A_in = _A_setup()

    def _B_setup():
        B, S, D, F = 1, 1, 576, 4608
        return {
            "x": _w((lambda *s: torch.randn(*s, dtype=torch.bfloat16) * 0.05)(B, S, D)),
            "w_gate": _w((lambda *s: torch.randn(*s, dtype=torch.bfloat16) * 0.05)(D, F)),
            "w_up": _w((lambda *s: torch.randn(*s, dtype=torch.bfloat16) * 0.05)(D, F)),
            "w_down": _w((lambda *s: torch.randn(*s, dtype=torch.bfloat16) * 0.05)(F, D)),
        }
    _B_in = _B_setup()

    return {
        "A_x": _A_in["x"],
        "A_k_cache": _A_in["k_cache"],
        "A_v_cache": _A_in["v_cache"],
        "A_wq": _A_in["wq"],
        "A_wo": _A_in["wo"],
        "B_x": _B_in["x"],
        "B_w_gate": _B_in["w_gate"],
        "B_w_up": _B_in["w_up"],
        "B_w_down": _B_in["w_down"],
    }

def reference(A_x, A_k_cache, A_v_cache, A_wq, A_wo, B_x, B_w_gate, B_w_up, B_w_down):
    def _A_ref(x, k_cache, v_cache, wq, wo):
        B, Sq, D = x.shape
        Hq, Dh = 32, 128
        Hkv = k_cache.shape[1]
        rep = Hq // Hkv
        q = (lambda x, *d: x.permute(*d))((x @ wq).reshape(B, Sq, Hq, Dh), 0, 2, 1, 3)
        k = (lambda x, n, axis: torch.repeat_interleave(x, n, dim=axis))(k_cache, rep, axis=1)
        v = (lambda x, n, axis: torch.repeat_interleave(x, n, dim=axis))(v_cache, rep, axis=1)
        scores = torch.einsum("bhqd,bhkd->bhqk", q, k) * (Dh ** -0.5)
        p = _softmax(scores, axis=-1)
        o = torch.einsum("bhqk,bhkd->bhqd", p, v)
        return (lambda x, *d: x.permute(*d))(o, 0, 2, 1, 3).reshape(B, Sq, Hq * Dh) @ wo

    def _B_ref(x, w_gate, w_up, w_down):
        g = _silu(x @ w_gate)
        u = x @ w_up
        return (g * u) @ w_down

    x = A_x
    xn = _layernorm(x, axis=-1)
    return x + _A_ref(xn, A_k_cache, A_v_cache, A_wq, A_wo) + _B_ref(xn, B_w_gate, B_w_up, B_w_down)
```

```cpp
#include <hip/hip_runtime.h>
#include <hip/hip_bf16.h>
#include <stdint.h>

typedef unsigned int u32;
typedef u32 u32x4 __attribute__((ext_vector_type(4)));

constexpr int D = 576, HQ = 32, HKV = 16, SK = 8192, DH = 128, F = 4608;
constexpr int NSPLIT = 16, KPS = SK / NSPLIT;

__device__ __forceinline__ float bf_lo(u32 x) { return __uint_as_float(x << 16); }
__device__ __forceinline__ float bf_hi(u32 x) { return __uint_as_float(x & 0xffff0000u); }
__device__ __forceinline__ float bf2f(unsigned short x) { return __uint_as_float(((u32)x) << 16); }

__device__ void layernorm_to_lds(const unsigned short* x, float* xn, float* red) {
  int t = threadIdx.x;
  float s = 0.f;
  for (int i = t; i < D; i += blockDim.x) { float v = bf2f(x[i]); xn[i] = v; s += v; }
  for (int o = 32; o; o >>= 1) s += __shfl_xor(s, o);
  if ((t & 63) == 0) red[t >> 6] = s;
  __syncthreads();
  float tot = 0.f;
  for (int w = 0; w < (int)(blockDim.x >> 6); ++w) tot += red[w];
  float mean = tot / D;
  __syncthreads();
  float v2 = 0.f;
  for (int i = t; i < D; i += blockDim.x) { float d = xn[i] - mean; v2 += d * d; }
  for (int o = 32; o; o >>= 1) v2 += __shfl_xor(v2, o);
  if ((t & 63) == 0) red[t >> 6] = v2;
  __syncthreads();
  float tv = 0.f;
  for (int w = 0; w < (int)(blockDim.x >> 6); ++w) tv += red[w];
  float rstd = 1.0f / sqrtf(tv / D + 1e-5f);
  for (int i = t; i < D; i += blockDim.x) xn[i] = (xn[i] - mean) * rstd;
  __syncthreads();
}

__device__ void gemv64(const unsigned short* W, int ldw, int col0, const float* xn, float* part  , float* outv) {
  int t = threadIdx.x, cg = t & 7, rg = t >> 3;
  float acc[8] = {0, 0, 0, 0, 0, 0, 0, 0};
  for (int r = rg; r < D; r += 32) {
    u32x4 w = *(const u32x4*)(W + (size_t)r * ldw + col0 + cg * 8);
    float xv = xn[r];
#pragma unroll
    for (int k = 0; k < 4; ++k) { acc[2 * k] += xv * bf_lo(w[k]); acc[2 * k + 1] += xv * bf_hi(w[k]); }
  }
#pragma unroll
  for (int k = 0; k < 8; ++k) part[rg * 64 + cg * 8 + k] = acc[k];
  __syncthreads();
  if (t < 64) { float s = 0.f; for (int g = 0; g < 32; ++g) s += part[g * 64 + t]; outv[t] = s; }
  __syncthreads();
}

__global__ void __launch_bounds__(256) k1(const unsigned short* x, const unsigned short* wq, const unsigned short* wg, const unsigned short* wu, float* q, float* h) {
  __shared__ float xn[D], red[4], part[32 * 64], o1[64], o2[64];
  layernorm_to_lds(x, xn, red);
  int b = blockIdx.x, t = threadIdx.x;
  if (b < 64) {
    gemv64(wq, HQ * DH, b * 64, xn, part, o1);
    if (t < 64) q[b * 64 + t] = o1[t];
  } else {
    int c = (b - 64) * 64;
    gemv64(wg, F, c, xn, part, o1);
    gemv64(wu, F, c, xn, part, o2);
    if (t < 64) { float g = o1[t]; h[c + t] = g / (1.f + __expf(-g)) * o2[t]; }
  }
}

__global__ void __launch_bounds__(256) k2(const unsigned short* kc, const unsigned short* vc, const float* q, float* pm, float* pl, float* po,
                                          const unsigned short* wd, const float* h, float* fpart) {
  __shared__ float sq[2][DH], sc[2][KPS], red[8], acc_s[16][2][DH];
  int b = blockIdx.x, t = threadIdx.x;
  if (b >= HKV * NSPLIT) {
    int fb = b - HKV * NSPLIT;
    float* part = &acc_s[0][0][0];
    int cg = t % 72, rg = t / 72;
    float acc[8] = {0, 0, 0, 0, 0, 0, 0, 0};
    if (rg < 3) {
      for (int r = rg; r < 64; r += 3) {
        int row = fb * 64 + r;
        u32x4 w = *(const u32x4*)(wd + (size_t)row * D + cg * 8);
        float hv = h[row];
#pragma unroll
        for (int k = 0; k < 4; ++k) { acc[2 * k] += hv * bf_lo(w[k]); acc[2 * k + 1] += hv * bf_hi(w[k]); }
      }
#pragma unroll
      for (int k = 0; k < 8; ++k) part[rg * D + cg * 8 + k] = acc[k];
    }
    __syncthreads();
    for (int i = t; i < D; i += 256) fpart[fb * D + i] = part[i] + part[D + i] + part[2 * D + i];
    return;
  }
  int kvh = b / NSPLIT, s = b % NSPLIT;
  sq[t >> 7][t & 127] = q[(kvh * 2 + (t >> 7)) * DH + (t & 127)];
  __syncthreads();
  const unsigned short* K = kc + ((size_t)kvh * SK + s * KPS) * DH;
  const unsigned short* V = vc + ((size_t)kvh * SK + s * KPS) * DH;
  int c = t & 15, r = t >> 4;
  float q0[8], q1[8];
#pragma unroll
  for (int k = 0; k < 8; ++k) { q0[k] = sq[0][c * 8 + k]; q1[k] = sq[1][c * 8 + k]; }
  for (int j = r; j < KPS; j += 16) {
    u32x4 w = *(const u32x4*)(K + (size_t)j * DH + c * 8);
    float a0 = 0.f, a1 = 0.f;
#pragma unroll
    for (int k = 0; k < 4; ++k) {
      a0 += q0[2 * k] * bf_lo(w[k]) + q0[2 * k + 1] * bf_hi(w[k]);
      a1 += q1[2 * k] * bf_lo(w[k]) + q1[2 * k + 1] * bf_hi(w[k]);
    }
    for (int o = 8; o; o >>= 1) { a0 += __shfl_xor(a0, o); a1 += __shfl_xor(a1, o); }
    if (c == 0) { sc[0][j] = a0 * 0.08838834764831845f; sc[1][j] = a1 * 0.08838834764831845f; }
  }
  __syncthreads();
  float m0 = fmaxf(sc[0][t], sc[0][t + 256]), m1 = fmaxf(sc[1][t], sc[1][t + 256]);
  for (int o = 32; o; o >>= 1) { m0 = fmaxf(m0, __shfl_xor(m0, o)); m1 = fmaxf(m1, __shfl_xor(m1, o)); }
  if ((t & 63) == 0) { red[t >> 6] = m0; red[4 + (t >> 6)] = m1; }
  __syncthreads();
  m0 = fmaxf(fmaxf(red[0], red[1]), fmaxf(red[2], red[3]));
  m1 = fmaxf(fmaxf(red[4], red[5]), fmaxf(red[6], red[7]));
  __syncthreads();
  float l0 = 0.f, l1 = 0.f;
  for (int j = t; j < KPS; j += 256) {
    float p0 = __expf(sc[0][j] - m0), p1 = __expf(sc[1][j] - m1);
    sc[0][j] = p0; sc[1][j] = p1; l0 += p0; l1 += p1;
  }
  for (int o = 32; o; o >>= 1) { l0 += __shfl_xor(l0, o); l1 += __shfl_xor(l1, o); }
  if ((t & 63) == 0) { red[t >> 6] = l0; red[4 + (t >> 6)] = l1; }
  __syncthreads();
  l0 = red[0] + red[1] + red[2] + red[3];
  l1 = red[4] + red[5] + red[6] + red[7];
  float a0[8] = {0, 0, 0, 0, 0, 0, 0, 0}, a1[8] = {0, 0, 0, 0, 0, 0, 0, 0};
  for (int j = r; j < KPS; j += 16) {
    u32x4 w = *(const u32x4*)(V + (size_t)j * DH + c * 8);
    float p0 = sc[0][j], p1 = sc[1][j];
#pragma unroll
    for (int k = 0; k < 4; ++k) {
      float lo = bf_lo(w[k]), hi = bf_hi(w[k]);
      a0[2 * k] += p0 * lo; a0[2 * k + 1] += p0 * hi;
      a1[2 * k] += p1 * lo; a1[2 * k + 1] += p1 * hi;
    }
  }
#pragma unroll
  for (int k = 0; k < 8; ++k) { acc_s[r][0][c * 8 + k] = a0[k]; acc_s[r][1][c * 8 + k] = a1[k]; }
  __syncthreads();
  {
    int hh = t >> 7, d = t & 127;
    float sum = 0.f;
    for (int g = 0; g < 16; ++g) sum += acc_s[g][hh][d];
    int head = kvh * 2 + hh;
    po[((size_t)head * NSPLIT + s) * DH + d] = sum;
    if (d == 0) { pm[head * NSPLIT + s] = hh ? m1 : m0; pl[head * NSPLIT + s] = hh ? l1 : l0; }
  }
}

__global__ void __launch_bounds__(256) k3(const float* pm, const float* pl, const float* po, const unsigned short* wo, float* opart) {
  __shared__ float o[DH], part[3 * D];
  int head = blockIdx.x, t = threadIdx.x;
  if (t < DH) {
    float M = -INFINITY;
    for (int s = 0; s < NSPLIT; ++s) M = fmaxf(M, pm[head * NSPLIT + s]);
    float L = 0.f, acc = 0.f;
    for (int s = 0; s < NSPLIT; ++s) {
      float w = __expf(pm[head * NSPLIT + s] - M);
      L += w * pl[head * NSPLIT + s];
      acc += w * po[((size_t)head * NSPLIT + s) * DH + t];
    }
    o[t] = acc / L;
  }
  __syncthreads();
  int cg = t % 72, rg = t / 72;
  float acc[8] = {0, 0, 0, 0, 0, 0, 0, 0};
  if (rg < 3) {
    for (int r = rg; r < DH; r += 3) {
      int row = head * DH + r;
      u32x4 w = *(const u32x4*)(wo + (size_t)row * D + cg * 8);
      float hv = o[r];
#pragma unroll
      for (int k = 0; k < 4; ++k) { acc[2 * k] += hv * bf_lo(w[k]); acc[2 * k + 1] += hv * bf_hi(w[k]); }
    }
#pragma unroll
    for (int k = 0; k < 8; ++k) part[rg * D + cg * 8 + k] = acc[k];
  }
  __syncthreads();
  for (int i = t; i < D; i += 256) opart[head * D + i] = part[i] + part[D + i] + part[2 * D + i];
}

__global__ void __launch_bounds__(576) k4(const unsigned short* x, const float* opart, const float* fpart, __hip_bfloat16* out) {
  int t = threadIdx.x;
  float a = 0.f, f = 0.f;
  for (int i = 0; i < HQ; ++i) a += opart[i * D + t];
  for (int i = 0; i < 72; ++i) f += fpart[i * D + t];
  out[t] = __float2bfloat16(bf2f(x[t]) + a + f);
}

extern "C" void kernel_launch(void* const* d_in, const int* in_sizes, int n_in, void* d_out, int out_size, void* d_ws, size_t ws_size,
                              hipStream_t stream) {
  const unsigned short* x = (const unsigned short*)d_in[0];
  const unsigned short* kc = (const unsigned short*)d_in[1];
  const unsigned short* vc = (const unsigned short*)d_in[2];
  const unsigned short* wq = (const unsigned short*)d_in[3];
  const unsigned short* wo = (const unsigned short*)d_in[4];
  const unsigned short* wg = (const unsigned short*)d_in[6];
  const unsigned short* wu = (const unsigned short*)d_in[7];
  const unsigned short* wd = (const unsigned short*)d_in[8];
  float* ws = (float*)d_ws;
  float* q = ws;
  float* h = q + 4096;
  float* pm = h + 4608;
  float* pl = pm + 512;
  float* po = pl + 512;
  float* fpart = po + 65536;
  float* opart = fpart + 72 * 576;
  k1<<<136, 256, 0, stream>>>(x, wq, wg, wu, q, h);
  k2<<<256 + 72, 256, 0, stream>>>(kc, vc, q, pm, pl, po, wd, h, fpart);
  k3<<<32, 256, 0, stream>>>(pm, pl, po, wo, opart);
  k4<<<1, 576, 0, stream>>>(x, opart, fpart, (__hip_bfloat16*)d_out);
}
```

```cpp
#include <hip/hip_runtime.h>
#include <hip/hip_bf16.h>
#include <stdint.h>

typedef unsigned int u32;
typedef u32 u32x4 __attribute__((ext_vector_type(4)));

constexpr int D = 576, HQ = 32, HKV = 16, SK = 8192, DH = 128, F = 4608;
constexpr int NSPLIT = 16, KPS = SK / NSPLIT;

__device__ __forceinline__ float bf_lo(u32 x) { return __uint_as_float(x << 16); }
__device__ __forceinline__ float bf_hi(u32 x) { return __uint_as_float(x & 0xffff0000u); }
__device__ __forceinline__ float bf2f(unsigned short x) { return __uint_as_float(((u32)x) << 16); }

__device__ __forceinline__ float rbf(float x) { return __bfloat162float(__float2bfloat16(x)); }
__device__ void layernorm_to_lds(const unsigned short* x, float* xn, float* red) {
  int t = threadIdx.x;
  float s = 0.f;
  for (int i = t; i < D; i += blockDim.x) { float v = bf2f(x[i]); xn[i] = v; s += v; }
  for (int o = 32; o; o >>= 1) s += __shfl_xor(s, o);
  if ((t & 63) == 0) red[t >> 6] = s;
  __syncthreads();
  float tot = 0.f;
  for (int w = 0; w < (int)(blockDim.x >> 6); ++w) tot += red[w];
  float mean = tot / D;
  __syncthreads();
  float v2 = 0.f;
  for (int i = t; i < D; i += blockDim.x) { float d = xn[i] - mean; v2 += d * d; }
  for (int o = 32; o; o >>= 1) v2 += __shfl_xor(v2, o);
  if ((t & 63) == 0) red[t >> 6] = v2;
  __syncthreads();
  float tv = 0.f;
  for (int w = 0; w < (int)(blockDim.x >> 6); ++w) tv += red[w];
  float mb = rbf(mean);
  float vb = rbf(tv / D);
  float den = rbf(sqrtf(rbf(vb + 1e-5f)));
  for (int i = t; i < D; i += blockDim.x) xn[i] = rbf(rbf(xn[i] - mb) / den);
  __syncthreads();
}

__device__ void gemv64(const unsigned short* W, int ldw, int col0, const float* xn, float* part  , float* outv) {
  int t = threadIdx.x, cg = t & 7, rg = t >> 3;
  float acc[8] = {0, 0, 0, 0, 0, 0, 0, 0};
  for (int r = rg; r < D; r += 32) {
    u32x4 w = *(const u32x4*)(W + (size_t)r * ldw + col0 + cg * 8);
    float xv = xn[r];
#pragma unroll
    for (int k = 0; k < 4; ++k) { acc[2 * k] += xv * bf_lo(w[k]); acc[2 * k + 1] += xv * bf_hi(w[k]); }
  }
#pragma unroll
  for (int k = 0; k < 8; ++k) part[rg * 64 + cg * 8 + k] = acc[k];
  __syncthreads();
  if (t < 64) { float s = 0.f; for (int g = 0; g < 32; ++g) s += part[g * 64 + t]; outv[t] = s; }
  __syncthreads();
}

__global__ void __launch_bounds__(256) k1(const unsigned short* x, const unsigned short* wq, const unsigned short* wg, const unsigned short* wu, float* q, float* h) {
  __shared__ float xn[D], red[4], part[32 * 64], o1[64], o2[64];
  layernorm_to_lds(x, xn, red);
  int b = blockIdx.x, t = threadIdx.x;
  if (b < 64) {
    gemv64(wq, HQ * DH, b * 64, xn, part, o1);
    if (t < 64) q[b * 64 + t] = rbf(o1[t]);
  } else {
    int c = (b - 64) * 64;
    gemv64(wg, F, c, xn, part, o1);
    gemv64(wu, F, c, xn, part, o2);
    if (t < 64) { float g = rbf(o1[t]); h[c + t] = rbf(rbf(g / (1.f + __expf(-g))) * rbf(o2[t])); }
  }
}

__global__ void __launch_bounds__(256) k2(const unsigned short* kc, const unsigned short* vc, const float* q, float* pm, float* pl, float* po,
                                          const unsigned short* wd, const float* h, float* fpart) {
  __shared__ float sq[2][DH], sc[2][KPS], red[8], acc_s[16][2][DH];
  int b = blockIdx.x, t = threadIdx.x;
  if (b >= HKV * NSPLIT) {
    int fb = b - HKV * NSPLIT;
    float* part = &acc_s[0][0][0];
    int cg = t % 72, rg = t / 72;
    float acc[8] = {0, 0, 0, 0, 0, 0, 0, 0};
    if (rg < 3) {
      for (int r = rg; r < 64; r += 3) {
        int row = fb * 64 + r;
        u32x4 w = *(const u32x4*)(wd + (size_t)row * D + cg * 8);
        float hv = h[row];
#pragma unroll
        for (int k = 0; k < 4; ++k) { acc[2 * k] += hv * bf_lo(w[k]); acc[2 * k + 1] += hv * bf_hi(w[k]); }
      }
#pragma unroll
      for (int k = 0; k < 8; ++k) part[rg * D + cg * 8 + k] = acc[k];
    }
    __syncthreads();
    for (int i = t; i < D; i += 256) fpart[fb * D + i] = part[i] + part[D + i] + part[2 * D + i];
    return;
  }
  int kvh = b / NSPLIT, s = b % NSPLIT;
  sq[t >> 7][t & 127] = q[(kvh * 2 + (t >> 7)) * DH + (t & 127)];
  __syncthreads();
  const unsigned short* K = kc + ((size_t)kvh * SK + s * KPS) * DH;
  const unsigned short* V = vc + ((size_t)kvh * SK + s * KPS) * DH;
  int c = t & 15, r = t >> 4;
  float q0[8], q1[8];
#pragma unroll
  for (int k = 0; k < 8; ++k) { q0[k] = sq[0][c * 8 + k]; q1[k] = sq[1][c * 8 + k]; }
  for (int j = r; j < KPS; j += 16) {
    u32x4 w = *(const u32x4*)(K + (size_t)j * DH + c * 8);
    float a0 = 0.f, a1 = 0.f;
#pragma unroll
    for (int k = 0; k < 4; ++k) {
      a0 += q0[2 * k] * bf_lo(w[k]) + q0[2 * k + 1] * bf_hi(w[k]);
      a1 += q1[2 * k] * bf_lo(w[k]) + q1[2 * k + 1] * bf_hi(w[k]);
    }
    for (int o = 8; o; o >>= 1) { a0 += __shfl_xor(a0, o); a1 += __shfl_xor(a1, o); }
    if (c == 0) { sc[0][j] = rbf(rbf(a0) * 0.08838834764831845f); sc[1][j] = rbf(rbf(a1) * 0.08838834764831845f); }
  }
  __syncthreads();
  float m0 = fmaxf(sc[0][t], sc[0][t + 256]), m1 = fmaxf(sc[1][t], sc[1][t + 256]);
  for (int o = 32; o; o >>= 1) { m0 = fmaxf(m0, __shfl_xor(m0, o)); m1 = fmaxf(m1, __shfl_xor(m1, o)); }
  if ((t & 63) == 0) { red[t >> 6] = m0; red[4 + (t >> 6)] = m1; }
  __syncthreads();
  m0 = fmaxf(fmaxf(red[0], red[1]), fmaxf(red[2], red[3]));
  m1 = fmaxf(fmaxf(red[4], red[5]), fmaxf(red[6], red[7]));
  __syncthreads();
  float l0 = 0.f, l1 = 0.f;
  for (int j = t; j < KPS; j += 256) {
    float p0 = __expf(sc[0][j] - m0), p1 = __expf(sc[1][j] - m1);
    sc[0][j] = p0; sc[1][j] = p1; l0 += p0; l1 += p1;
  }
  for (int o = 32; o; o >>= 1) { l0 += __shfl_xor(l0, o); l1 += __shfl_xor(l1, o); }
  if ((t & 63) == 0) { red[t >> 6] = l0; red[4 + (t >> 6)] = l1; }
  __syncthreads();
  l0 = red[0] + red[1] + red[2] + red[3];
  l1 = red[4] + red[5] + red[6] + red[7];
  float a0[8] = {0, 0, 0, 0, 0, 0, 0, 0}, a1[8] = {0, 0, 0, 0, 0, 0, 0, 0};
  for (int j = r; j < KPS; j += 16) {
    u32x4 w = *(const u32x4*)(V + (size_t)j * DH + c * 8);
    float p0 = sc[0][j], p1 = sc[1][j];
#pragma unroll
    for (int k = 0; k < 4; ++k) {
      float lo = bf_lo(w[k]), hi = bf_hi(w[k]);
      a0[2 * k] += p0 * lo; a0[2 * k + 1] += p0 * hi;
      a1[2 * k] += p1 * lo; a1[2 * k + 1] += p1 * hi;
    }
  }
#pragma unroll
  for (int k = 0; k < 8; ++k) { acc_s[r][0][c * 8 + k] = a0[k]; acc_s[r][1][c * 8 + k] = a1[k]; }
  __syncthreads();
  {
    int hh = t >> 7, d = t & 127;
    float sum = 0.f;
    for (int g = 0; g < 16; ++g) sum += acc_s[g][hh][d];
    int head = kvh * 2 + hh;
    po[((size_t)head * NSPLIT + s) * DH + d] = sum;
    if (d == 0) { pm[head * NSPLIT + s] = hh ? m1 : m0; pl[head * NSPLIT + s] = hh ? l1 : l0; }
  }
}

__global__ void __launch_bounds__(256) k3(const float* pm, const float* pl, const float* po, const unsigned short* wo, float* opart) {
  __shared__ float o[DH], part[3 * D];
  int head = blockIdx.x, t = threadIdx.x;
  if (t < DH) {
    float M = -INFINITY;
    for (int s = 0; s < NSPLIT; ++s) M = fmaxf(M, pm[head * NSPLIT + s]);
    float L = 0.f, acc = 0.f;
    for (int s = 0; s < NSPLIT; ++s) {
      float w = __expf(pm[head * NSPLIT + s] - M);
      L += w * pl[head * NSPLIT + s];
      acc += w * po[((size_t)head * NSPLIT + s) * DH + t];
    }
    o[t] = rbf(acc / L);
  }
  __syncthreads();
  int cg = t % 72, rg = t / 72;
  float acc[8] = {0, 0, 0, 0, 0, 0, 0, 0};
  if (rg < 3) {
    for (int r = rg; r < DH; r += 3) {
      int row = head * DH + r;
      u32x4 w = *(const u32x4*)(wo + (size_t)row * D + cg * 8);
      float hv = o[r];
#pragma unroll
      for (int k = 0; k < 4; ++k) { acc[2 * k] += hv * bf_lo(w[k]); acc[2 * k + 1] += hv * bf_hi(w[k]); }
    }
#pragma unroll
    for (int k = 0; k < 8; ++k) part[rg * D + cg * 8 + k] = acc[k];
  }
  __syncthreads();
  for (int i = t; i < D; i += 256) opart[head * D + i] = part[i] + part[D + i] + part[2 * D + i];
}

__global__ void __launch_bounds__(576) k4(const unsigned short* x, const float* opart, const float* fpart, __hip_bfloat16* out) {
  int t = threadIdx.x;
  float a = 0.f, f = 0.f;
  for (int i = 0; i < HQ; ++i) a += opart[i * D + t];
  for (int i = 0; i < 72; ++i) f += fpart[i * D + t];
  out[t] = __float2bfloat16(rbf(bf2f(x[t]) + rbf(a)) + rbf(f));
}

extern "C" void kernel_launch(void* const* d_in, const int* in_sizes, int n_in, void* d_out, int out_size, void* d_ws, size_t ws_size,
                              hipStream_t stream) {
  const unsigned short* x = (const unsigned short*)d_in[0];
  const unsigned short* kc = (const unsigned short*)d_in[1];
  const unsigned short* vc = (const unsigned short*)d_in[2];
  const unsigned short* wq = (const unsigned short*)d_in[3];
  const unsigned short* wo = (const unsigned short*)d_in[4];
  const unsigned short* wg = (const unsigned short*)d_in[6];
  const unsigned short* wu = (const unsigned short*)d_in[7];
  const unsigned short* wd = (const unsigned short*)d_in[8];
  float* ws = (float*)d_ws;
  float* q = ws;
  float* h = q + 4096;
  float* pm = h + 4608;
  float* pl = pm + 512;
  float* po = pl + 512;
  float* fpart = po + 65536;
  float* opart = fpart + 72 * 576;
  k1<<<136, 256, 0, stream>>>(x, wq, wg, wu, q, h);
  k2<<<256 + 72, 256, 0, stream>>>(kc, vc, q, pm, pl, po, wd, h, fpart);
  k3<<<32, 256, 0, stream>>>(pm, pl, po, wo, opart);
  k4<<<1, 576, 0, stream>>>(x, opart, fpart, (__hip_bfloat16*)d_out);
}
```

```cpp
#include <hip/hip_runtime.h>
#include <stdint.h>

#define GKS 3
#define CPRIO 3
#define APRIO 2
#define DLT 64
#define DLG (DLT * 27 / 32)
#define GU0 20
#define KV0 (GU0 + 46 * DLG / 16)
#define TPRE 4
#define VGAP 24
#define PVLAG 3
#define TQ 340
#define TWARM 100
#define MAPX 0
#define RDEL 80
#define DBG 0

#define GAS __attribute__((address_space(1)))
#define LAS __attribute__((address_space(3)))
typedef unsigned u32;
typedef unsigned long long u64;
typedef unsigned short u16;
typedef u32 v4u __attribute__((ext_vector_type(4)));
typedef u32 v2u __attribute__((ext_vector_type(2)));
typedef float f32x4 __attribute__((ext_vector_type(4)));
typedef float f32x2 __attribute__((ext_vector_type(2)));
typedef __bf16 bf16x8 __attribute__((ext_vector_type(8)));
typedef __bf16 bf16x2 __attribute__((ext_vector_type(2)));
typedef __amdgpu_buffer_rsrc_t rsrc_t;

constexpr int D = 576, SK = 8192, DH = 128, FF = 4608, QD = 4096;
constexpr int NWG = 256, NTHR = 768;
constexpr int NRED = 36, RED0 = NWG - NRED;
constexpr int NGU = 72 * GKS;
constexpr int GG = 18 / GKS;
constexpr int WS_EPOCH = 0;
constexpr int WS_Q = 4096;
constexpr int WS_REC = WS_Q + 16 * 4 * 128 * 16;
constexpr int WS_H = WS_REC + NWG * 16 * 8 * 16;
constexpr int WS_PART = WS_H + GKS * FF * 16;
constexpr int WS_END = WS_PART + NRED * NWG * 128;
constexpr int WS_DBG = 4 << 20;
constexpr u32 SPIN_CAP = 1u << 15;
constexpr int OOB = 0x40000000;
constexpr int L_FLAG = 0;
constexpr int L_TAG = 16;
constexpr int L_Q = 32;
constexpr int L_MX = L_Q + 128;
constexpr int L_LS = L_MX + 16;
constexpr int L_ORED = L_LS + 16;
constexpr int L_XT = L_ORED + 8 * 256;
constexpr int L_QP = L_XT + 640;
constexpr int L_GU = L_QP + 512;
constexpr int L_FIN = L_GU + 512;
constexpr int L_HV = L_FIN + 128;
constexpr int L_OV = L_HV + 32;
constexpr int L_WORDS = L_OV + 16;
enum { F_QRDY = 0, F_TRIG = 1, F_MAXC = 2, F_DONE = 3, F_GU = 4, F_PUB = 5, F_FIN = 6, F_XN = 7, F_QP = 8, F_LS = 9, F_H = 10, F_O = 11 };

__device__ __forceinline__ float u2f(u32 x) { return __builtin_bit_cast(float, x); }
__device__ __forceinline__ u32 f2u(float x) { return __builtin_bit_cast(u32, x); }
__device__ __forceinline__ float bf_lo(u32 x) { return u2f(x << 16); }
__device__ __forceinline__ float bf_hi(u32 x) { return u2f(x & 0xffff0000u); }
__device__ __forceinline__ u32 cvtpk(float lo, float hi) {
  f32x2 v = {lo, hi};
  return __builtin_bit_cast(u32, __builtin_convertvector(v, bf16x2));
}
__device__ __forceinline__ float rbf(float x) { return (float)(__bf16)x; }
__device__ __forceinline__ float rcp(float x) { return __builtin_amdgcn_rcpf(x); }
__device__ __forceinline__ float opq(float x) { asm volatile("" : "+v"(x)); return x; }
template <int CTRL> __device__ __forceinline__ u32 dppu(u32 x) { return (u32)__builtin_amdgcn_mov_dpp((int)x, CTRL, 0xf, 0xf, true); }
template <int CTRL> __device__ __forceinline__ float dpp(float x) { return u2f(dppu<CTRL>(f2u(x))); }
constexpr int XOR1 = 0xB1, XOR2 = 0x4E, HMIR = 0x141, XOR8 = 0x128;
__device__ __forceinline__ float xrow_max(float x) {
  auto s = __builtin_amdgcn_permlane16_swap(f2u(x), f2u(x), false, false);
  x = fmaxf(u2f(s[0]), u2f(s[1]));
  auto t = __builtin_amdgcn_permlane32_swap(f2u(x), f2u(x), false, false);
  return fmaxf(u2f(t[0]), u2f(t[1]));
}
__device__ __forceinline__ float xrow_sum(float x) {
  auto s = __builtin_amdgcn_permlane16_swap(f2u(x), f2u(x), false, false);
  x = u2f(s[0]) + u2f(s[1]);
  auto t = __builtin_amdgcn_permlane32_swap(f2u(x), f2u(x), false, false);
  return u2f(t[0]) + u2f(t[1]);
}
__device__ __forceinline__ float row_sum(float x) {
  x = opq(x);
  x += dpp<XOR1>(x); x += dpp<XOR2>(x); x += dpp<HMIR>(x); x += dpp<XOR8>(x); return x;
}
__device__ __forceinline__ float row_max(float x) {
  x = fmaxf(x, dpp<XOR1>(x)); x = fmaxf(x, dpp<XOR2>(x)); x = fmaxf(x, dpp<HMIR>(x)); x = fmaxf(x, dpp<XOR8>(x)); return x;
}
__device__ __forceinline__ f32x4 mfma(v4u a, v4u b, f32x4 c) {
  return __builtin_amdgcn_mfma_f32_16x16x32_bf16(__builtin_bit_cast(bf16x8, a), __builtin_bit_cast(bf16x8, b), c, 0, 0, 0);
}
template <int ODD> __device__ __forceinline__ f32x2 fold(f32x4 a) {
  auto s0 = __builtin_amdgcn_permlane32_swap(f2u(a[0]), f2u(a[2]), false, false);
  auto s1 = __builtin_amdgcn_permlane32_swap(f2u(a[1]), f2u(a[3]), false, false);
  f32x2 r;
  if (ODD) { r[0] = dpp<XOR8>(u2f(s0[0])) + u2f(s0[1]); r[1] = dpp<XOR8>(u2f(s1[0])) + u2f(s1[1]); }
  else { r[0] = u2f(s0[0]) + dpp<XOR8>(u2f(s0[1])); r[1] = u2f(s1[0]) + dpp<XOR8>(u2f(s1[1])); }
  return r;
}
#define LGKM0() asm volatile("s_waitcnt lgkmcnt(0)" ::: "memory")
#define SCHEDB() __builtin_amdgcn_sched_barrier(0)
template <int N> __device__ __forceinline__ void vmwait() { asm volatile("s_waitcnt vmcnt(%0)" ::"i"(N) : "memory"); }
__device__ __forceinline__ u32 lds_ld(LAS u32* p) { return __hip_atomic_load(p, __ATOMIC_RELAXED, __HIP_MEMORY_SCOPE_WORKGROUP); }
__device__ __forceinline__ void lds_wait_ge(LAS u32* p, u32 n) {
#pragma unroll 1
  for (u32 i = 0; i < (1u << 20); ++i) {
    if ((u32)__builtin_amdgcn_readfirstlane(lds_ld(p)) >= n) break;
    __builtin_amdgcn_s_sleep(1);
  }
  asm volatile("" ::: "memory");
}
__device__ __forceinline__ u32 lds_arrive(LAS u32* p, int lane) {
  LGKM0();
  u32 r = 0;
  if (lane == 0) r = __hip_atomic_fetch_add(p, 1u, __ATOMIC_RELAXED, __HIP_MEMORY_SCOPE_WORKGROUP);
  r = __builtin_amdgcn_readfirstlane(r);
  asm volatile("" ::: "memory");
  return r;
}
__device__ __forceinline__ void lds_signal(LAS u32* p, int lane) {
  LGKM0();
  if (lane == 0) __hip_atomic_fetch_add(p, 1u, __ATOMIC_RELAXED, __HIP_MEMORY_SCOPE_WORKGROUP);
  asm volatile("" ::: "memory");
}
__device__ __forceinline__ u32 now() { return (u32)__builtin_amdgcn_s_memrealtime(); }
template <int SL> __device__ __forceinline__ void wait_sl(u32 t) {
  asm volatile("s_branch .Lwt%=\n"
               ".Lws%=:\n\ts_sleep %1\n"
               ".Lwt%=:\n\ts_memrealtime vcc\n\ts_waitcnt lgkmcnt(0)\n\ts_sub_i32 vcc_hi, %0, vcc_lo\n\ts_cmp_gt_i32 vcc_hi, 0\n\ts_cbranch_scc1 .Lws%="
               : : "s"(t), "i"(SL) : "memory", "scc", "vcc");
}
__device__ __forceinline__ void wait_until(u32 t) { wait_sl<1>(t); }
__device__ __forceinline__ void wait_long(u32 t) { wait_sl<12>(t - 45); wait_sl<1>(t); }
template <int I> struct IC { static constexpr int value = I; };
template <int I, int N, class F> __device__ __forceinline__ void static_for(F&& f) {
  if constexpr (I < N) { f(IC<I>{}); static_for<I + 1, N>(f); }
}
constexpr int QK0 = 16 + PVLAG - 1;
constexpr int pv_done(int i) {
  if (i < QK0 + 6) return 0;
  if (i >= 32) return 16;
  const int p = pv_done(i - 1), a = i - 16 - PVLAG + 1;
  return a < p ? p : (a < p + 3 ? a : p + 3);
}
constexpr int AUX_SC1 = 16, AUX_NT = 2;
__device__ __forceinline__ rsrc_t mkrs(const void* p, int bytes) { return __builtin_amdgcn_make_buffer_rsrc((void*)p, 0, bytes, 0x00020000); }
__device__ __forceinline__ v4u ld_nt(rsrc_t r, int voff, int soff) { return __builtin_amdgcn_raw_buffer_load_b128(r, voff, soff, AUX_NT); }
__device__ __forceinline__ v4u ld_sc1(rsrc_t rs, int off) { return __builtin_amdgcn_raw_buffer_load_b128(rs, off, 0, AUX_SC1); }
__device__ __forceinline__ void st_pair(rsrc_t rs, int off, u32 a, u32 b, u32 T0, u32 T1) {
  v4u v = {a, T0, b, T1};
  __builtin_amdgcn_raw_buffer_store_b128(v, rs, off, 0, AUX_SC1);
}
#define STAMP(k) do { } while (0)
#define FLUSH() do { } while (0)

__global__ void __launch_bounds__(NTHR, 3)
fused_kernel(const u16* __restrict__ x_, const u16* __restrict__ kc_, const u16* __restrict__ vc_, const u16* __restrict__ wq_,
             const u16* __restrict__ wo_, const u16* __restrict__ wg_, const u16* __restrict__ wu_, const u16* __restrict__ wd_,
             u16* __restrict__ out_, unsigned char* ws_) {
  __shared__ __attribute__((aligned(16))) u32 lds_raw[L_WORDS];
  LAS u32* L = (LAS u32*)lds_raw;
  asm volatile("" ::"s"(x_), "s"(kc_), "s"(vc_), "s"(wq_), "s"(wo_), "s"(wg_), "s"(wu_), "s"(wd_), "s"(out_), "s"(ws_));
  const u32 t0 = now();
  const int b = blockIdx.x;
  const int lane = threadIdx.x & 63;
  const int wave = __builtin_amdgcn_readfirstlane(threadIdx.x >> 6);
  const int h = (b & 7) * 2 + ((b >> 3) & 1), s = b >> 4;
  const int cw = wave - 8;
  const rsrc_t rs = mkrs(ws_, WS_END);
  v4u mk;
  {
    const int e = lane & 7;
    const u32 hv = (e & 1) ? 0xffff0000u : 0x0000ffffu;
    mk[0] = (e >> 1) == 0 ? hv : 0u; mk[1] = (e >> 1) == 1 ? hv : 0u; mk[2] = (e >> 1) == 2 ? hv : 0u; mk[3] = (e >> 1) == 3 ? hv : 0u;
  }
  const int par = (lane >> 3) & 1;
  LAS u32* xt = L + L_XT;

  if (__builtin_expect(wave < 8, 1)) {
    if (wave == 0) STAMP(0);
    v4u K[16], V[16];
    const size_t base = ((size_t)(h * SK + s * 512) * DH) * 2;
    const rsrc_t rk = mkrs((const char*)kc_ + base, 512 * DH * 2), rv = mkrs((const char*)vc_ + base, 512 * DH * 2);
    const int ko = wave * 1024 + (((lane >> 2) & 3) * 16 + (lane >> 4) * 4 + (lane & 3)) * 16;
    const int vo = wave * 1024 + lane * 16;
    const u32 tb = t0 + KV0 + ((wave * DLT) >> 4);
    const int hb = par;
    const bool t1 = (lane >> 2) & 1;
    v4u Be, Bo;
    float sc[8];
    u32 pk[8], tw[2], T0 = 0, T1 = 0;
    float m = -INFINITY;
    f32x4 acc0 = {0.f, 0.f, 0.f, 0.f}, acc1 = {0.f, 0.f, 0.f, 0.f};
    __builtin_amdgcn_s_setprio(APRIO);
    __builtin_amdgcn_s_barrier();
    static_for<0, 33>([&](auto ic) __attribute__((always_inline)) {
      constexpr int i = decltype(ic)::value;
      if constexpr (i < 32) {
        if constexpr (i == 0) wait_long(tb);
        else wait_until(tb + ((i * DLT) >> 1) + (i >= VGAP ? (3 * TPRE * DLT) >> 4 : 0));
        if constexpr (i < 16) K[i] = ld_nt(rk, ko, i * 8192); else V[i - 16] = ld_nt(rv, vo, (i - 16) * 8192);
        SCHEDB();
      }
      if (i == 31 && wave == 7) STAMP(1);
      if constexpr (i == QK0) {
        lds_wait_ge(L + L_FLAG + F_QRDY, 1);
        if (wave == 0) STAMP(2);
        const v4u qf = *(const LAS v4u*)(L + L_Q + hb * 64 + (lane >> 4) * 16 + (lane & 3) * 4);
        const v4u zero4 = {0u, 0u, 0u, 0u};
        Be = t1 ? zero4 : qf; Bo = t1 ? qf : zero4;
      }
      if constexpr (i >= QK0 && i < QK0 + 4) {
#pragma unroll
        for (int j = 2 * (i - QK0); j < 2 * (i - QK0) + 2; ++j) {
          f32x4 d = {0.f, 0.f, 0.f, 0.f};
          d = mfma(K[2 * j], Be, d);
          d = mfma(K[2 * j + 1], Bo, d);
          float v = (dpp<0x00>(d[0]) + dpp<0x55>(d[1])) + (dpp<0xAA>(d[2]) + dpp<0xFF>(d[3]));
          v = rbf(rbf(v) * 0.08838834764831845f);
          sc[j] = v;
          m = fmaxf(m, v);
        }
      }
      if constexpr (i == QK0 + 3) {
        m = fmaxf(m, dpp<HMIR>(m));
        m = xrow_max(m);
        if ((lane & 55) == 0) L[L_MX + hb * 8 + wave] = f2u(m);
        lds_signal(L + L_FLAG + F_MAXC, lane);
      }
      if constexpr (i == QK0 + 5) {
        lds_wait_ge(L + L_FLAG + F_MAXC, 8);
        const f32x4 a = *(const LAS f32x4*)(L + L_MX + hb * 8), c = *(const LAS f32x4*)(L + L_MX + hb * 8 + 4);
        const float M = fmaxf(fmaxf(fmaxf(a[0], a[1]), fmaxf(a[2], a[3])), fmaxf(fmaxf(c[0], c[1]), fmaxf(c[2], c[3])));
        float ls = 0.f;
#pragma unroll
        for (int j = 0; j < 8; ++j) {
          const float p = __expf(sc[j] - M);
          ls += p;
          pk[j] = cvtpk(p, p);
        }
        ls += dpp<HMIR>(ls);
        ls = xrow_sum(ls);
        if ((lane & 55) == 0) L[L_LS + hb * 8 + wave] = f2u(ls);
        lds_signal(L + L_FLAG + F_LS, lane);
        if (wave == 0) STAMP(3);
      }
      if constexpr (i == QK0 + 8) {
        lds_wait_ge(L + L_FLAG + F_LS, 8);
        const v2u tg = *(const LAS v2u*)(L + L_TAG);
        T0 = __builtin_amdgcn_readfirstlane(tg[0]); T1 = __builtin_amdgcn_readfirstlane(tg[1]);
#pragma unroll
        for (int hh = 0; hh < 2; ++hh) {
          const LAS u32* sp = L + ((lane & 4) ? L_LS : L_MX) + hh * 8;
          const f32x4 a = *(const LAS f32x4*)sp, c = *(const LAS f32x4*)(sp + 4);
          const float Mh = fmaxf(fmaxf(fmaxf(a[0], a[1]), fmaxf(a[2], a[3])), fmaxf(fmaxf(c[0], c[1]), fmaxf(c[2], c[3])));
          const float Lh = ((a[0] + a[1]) + (a[2] + a[3])) + ((c[0] + c[1]) + (c[2] + c[3]));
          tw[hh] = (T1 & 0xffffff00u) | ((f2u((lane & 4) ? Lh : Mh) >> ((lane & 3) * 8)) & 0xffu);
        }
      }
      if constexpr (i >= QK0 + 6) {
        constexpr int lo = pv_done(i - 1), hi = pv_done(i);
#pragma unroll
        for (int j = lo; j < hi; ++j) {
          const u32 pp = pk[j >> 1], pm = dppu<HMIR>(pp);
          if (j & 1) acc1 = mfma(v4u{pm & mk[0], pm & mk[1], pp & mk[2], pp & mk[3]}, V[j], acc1);
          else acc0 = mfma(v4u{pp & mk[0], pp & mk[1], pm & mk[2], pm & mk[3]}, V[j], acc0);
        }
      }
      SCHEDB();
    });
    acc0 += acc1;
    *(LAS f32x4*)(L + L_ORED + wave * 256 + (lane >> 5) * 128 + 8 * (lane & 15) + 4 * ((lane >> 4) & 1)) = acc0;
    if (wave == 7) STAMP(15);
    const u32 old = lds_arrive(L + L_FLAG + F_DONE, lane);
    if (old == 7) {
      f32x2 r[2][8];
#pragma unroll
      for (int k = 0; k < 8; ++k) {
        r[0][k] = *(const LAS f32x2*)(L + L_ORED + k * 256 + 2 * lane);
        r[1][k] = *(const LAS f32x2*)(L + L_ORED + k * 256 + 128 + 2 * lane);
      }
#pragma unroll
      for (int hh = 0; hh < 2; ++hh) {
        const f32x2 o = ((r[hh][0] + r[hh][1]) + (r[hh][2] + r[hh][3])) + ((r[hh][4] + r[hh][5]) + (r[hh][6] + r[hh][7]));
        const int rec = (h * 16 + hh * 8 + (lane >> 3)) * 16 + s;
        st_pair(rs, WS_REC + (rec * 8 + (lane & 7)) * 16, f2u(o[0]), f2u(o[1]), T0, tw[hh]);
      }
      STAMP(4);
    }
    if (b >= RED0) {
      const int r = b - RED0;
      const int col = 64 * (r >> 2) + 4 * ((r >> 1) & 1) + 2 * (r & 1) + 8 * ((lane >> 1) & 7) + (lane & 1);
      const u32 xo = x_[col];
      const int pb = WS_PART + (r * NWG + wave * 32) * 128 + lane * 16;
      v4u g[4];
      bool ok[4];
#pragma unroll
      for (int k = 0; k < 4; ++k) { g[k] = v4u{0u, 0u, 0u, 0u}; ok[k] = false; }
#pragma unroll 1
      for (u32 it = 0; it < SPIN_CAP; ++it) {
#pragma unroll
        for (int k = 0; k < 4; ++k)
          if (!ok[k]) g[k] = ld_sc1(rs, pb + k * 1024);
        bool all = true;
#pragma unroll
        for (int k = 0; k < 4; ++k) { ok[k] = ok[k] || (g[k][1] == T0 && g[k][3] == T1); all = all && ok[k]; }
        if (__all(all)) break;
        if (it == 0) { lds_wait_ge(L + L_FLAG + F_PUB, 3); if (wave == 0) STAMP(12); }
        asm volatile("" ::: "memory");
      }
      float a0 = (u2f(g[0][0]) + u2f(g[1][0])) + (u2f(g[2][0]) + u2f(g[3][0]));
      float a1 = (u2f(g[0][2]) + u2f(g[1][2])) + (u2f(g[2][2]) + u2f(g[3][2]));
      a0 += dpp<XOR8>(a0); a1 += dpp<XOR8>(a1);
      a0 = xrow_sum(a0); a1 = xrow_sum(a1);
      if (lane < 8) *(LAS v2u*)(L + L_FIN + wave * 16 + 2 * lane) = v2u{f2u(a0), f2u(a1)};
      const u32 old2 = lds_arrive(L + L_FLAG + F_FIN, lane);
      if (old2 == 7) {
        if (lane < 16) {
          const LAS float* f = (const LAS float*)(L + L_FIN) + lane;
          const float A = ((f[0] + f[16]) + (f[32] + f[48])) + ((f[64] + f[80]) + (f[96] + f[112]));
          const float o = bf_lo(xo) + A;
          out_[col] = (u16)cvtpk(o, o);
        }
        if (r == 0 && lane == 0) {
          const v2u e1 = {T0, T1 - T0 * 0x9E3779B1u};
          __builtin_amdgcn_raw_buffer_store_b64(e1, rs, WS_EPOCH, 0, AUX_SC1);
        }
        STAMP(13);
      }
    }
    FLUSH();
    return;
  }

  __builtin_amdgcn_s_setprio(CPRIO);
  const bool hasgu = b < NGU;
  const int gcg = b / GKS, gks = b % GKS;
  const int grp0 = (72 / GKS) * gks + GG * cw;
  const rsrc_t rg = mkrs(wg_, D * FF * 2), ru = mkrs(wu_, D * FF * 2);
  const int go = (grp0 * 8 + (lane >> 3)) * (FF * 2) + gcg * 128 + (lane & 7) * 16 + (hasgu ? 0 : OOB);
  v4u Wg[GG], Wu[GG];
  u32 T0, T1;
  const u32 tg0 = t0 + GU0 + (((cw ? cw - 1 : 6 * GG) * DLG) >> 4);
  const int gstep = cw ? 3 * DLG : DLG;
#define GULOADS() do { _Pragma("unroll") for (int i = 0; i < GG; ++i) { \
    wait_until(tg0 + ((2 * i * gstep) >> 4)); Wg[i] = ld_nt(rg, go, i * 8 * FF * 2); SCHEDB(); \
    wait_until(tg0 + (((2 * i + 1) * gstep) >> 4)); Wu[i] = ld_nt(ru, go, i * 8 * FF * 2); SCHEDB(); } } while (0)
  if (cw == 0) {
    const v4u xa = *((const GAS v4u*)x_ + lane);
    const v4u xb = *((const GAS v4u*)x_ + 64 + (lane & 7));
    const u64 ep = __hip_atomic_load((GAS u64*)(ws_ + WS_EPOCH), __ATOMIC_RELAXED, __HIP_MEMORY_SCOPE_AGENT);
    SCHEDB();
    if (lane < 16) L[L_FLAG + lane] = 0u;
    LGKM0();
    __builtin_amdgcn_s_barrier();
    float v[16];
#pragma unroll
    for (int k = 0; k < 4; ++k) { v[2 * k] = bf_lo(xa[k]); v[2 * k + 1] = bf_hi(xa[k]); v[8 + 2 * k] = bf_lo(xb[k]); v[9 + 2 * k] = bf_hi(xb[k]); }
    const bool tail = lane < 8;
    float s0 = ((v[0] + v[1]) + (v[2] + v[3])) + ((v[4] + v[5]) + (v[6] + v[7]));
    float s1 = ((v[8] + v[9]) + (v[10] + v[11])) + ((v[12] + v[13]) + (v[14] + v[15]));
    const float mean = xrow_sum(row_sum(s0 + (tail ? s1 : 0.f))) * (1.f / D);
    float q0 = 0.f, q1 = 0.f;
#pragma unroll
    for (int k = 0; k < 8; ++k) { float d0 = v[k] - mean, d1 = v[8 + k] - mean; q0 += d0 * d0; q1 += d1 * d1; }
    const float var = xrow_sum(row_sum(q0 + (tail ? q1 : 0.f))) * (1.f / D);
    const float mb = rbf(mean), vb = rbf(var);
    const float rden = rcp(rbf(__builtin_amdgcn_sqrtf(rbf(vb + 1e-5f))));
#pragma unroll
    for (int k = 0; k < 8; ++k) {
      const float a = rbf(v[k] - mb) * rden;
      xt[k * 80 + lane] = cvtpk(a, a);
    }
    if (tail) {
#pragma unroll
      for (int k = 0; k < 8; ++k) {
        const float a = rbf(v[8 + k] - mb) * rden;
        xt[k * 80 + 64 + lane] = cvtpk(a, a);
      }
    }
    const u64 e1 = ep + 1;
    T0 = __builtin_amdgcn_readfirstlane((u32)e1);
    T1 = __builtin_amdgcn_readfirstlane((u32)(e1 >> 32)) + T0 * 0x9E3779B1u;
    if (lane == 0) *(LAS v2u*)(L + L_TAG) = v2u{T0, T1};
    lds_signal(L + L_FLAG + F_XN, lane);
    STAMP(14);
    GULOADS();
  } else {
    const int r0 = 144 * (s & 3) + 48 * (cw - 1);
    const rsrc_t rq = mkrs(wq_, D * QD * 2);
    const int qo = (r0 + (lane >> 3)) * (QD * 2) + (256 * h + 64 * (s >> 2)) * 2 + (lane & 7) * 16;
    v4u Wq[6];
#pragma unroll
    for (int i = 0; i < 6; ++i) Wq[i] = ld_nt(rq, qo, i * 8 * QD * 2);
    SCHEDB();
    __builtin_amdgcn_s_barrier();
    GULOADS();
    lds_wait_ge(L + L_FLAG + F_XN, 1);
    const LAS u32* xr = xt + (lane >> 3) * 80 + (r0 >> 3);
    const v2u tg = *(const LAS v2u*)(L + L_TAG);
    f32x4 a0 = {0.f, 0.f, 0.f, 0.f}, a1 = {0.f, 0.f, 0.f, 0.f};
#pragma unroll
    for (int i = 0; i < 6; i += 2) {
      const v2u xv = *(const LAS v2u*)(xr + i);
      a0 = mfma(xv[0] & mk, Wq[i], a0);
      a1 = mfma(xv[1] & mk, Wq[i + 1], a1);
    }
    a0 += a1;
    T0 = __builtin_amdgcn_readfirstlane(tg[0]);
    T1 = __builtin_amdgcn_readfirstlane(tg[1]);
    if ((lane >> 5) == par) {
      LAS u32* g = L + L_QP + (8 * (lane & 7) + 4 * ((lane >> 4) & 1)) * 8 + (cw - 1) * 2 + par;
#pragma unroll
      for (int r = 0; r < 4; ++r) g[r * 8] = f2u(a0[r]);
    }
    const u32 old = lds_arrive(L + L_FLAG + F_QP, lane);
    if (old == 2) {
      const LAS u32* qp = L + L_QP + (lane & 31) * 16;
      const f32x4 a = *(const LAS f32x4*)qp, a2 = *(const LAS f32x4*)(qp + 8);
      const f32x2 c = *(const LAS f32x2*)(qp + 4), c2 = *(const LAS f32x2*)(qp + 12);
      const float q = ((a[0] + a[1]) + (a[2] + a[3])) + (c[0] + c[1]);
      const float qn = ((a2[0] + a2[1]) + (a2[2] + a2[3])) + (c2[0] + c2[1]);
      if (lane < 32) st_pair(rs, WS_Q + ((h * 4 + (s & 3)) * 128 + 32 * (s >> 2) + lane) * 16, f2u(q), f2u(qn), T0, T1);
      STAMP(5);
    }
  }
  if (hasgu) {
    const LAS u32* xr = xt + (lane >> 3) * 80 + grp0;
    f32x4 ag = {0.f, 0.f, 0.f, 0.f}, au = {0.f, 0.f, 0.f, 0.f};
#pragma unroll
    for (int i = 0; i < GG; ++i) {
      const v4u A = xr[i] & mk;
      ag = mfma(A, Wg[i], ag);
      au = mfma(A, Wu[i], au);
    }
    const f32x2 fg = fold<0>(ag), fu = fold<1>(au);
    const int c = 8 * (lane & 7) + 4 * ((lane >> 4) & 1) + 2 * (lane >> 5);
    LAS u32* g = L + L_GU + (par * 64 + c) * 4 + cw;
    g[0] = f2u(par ? fu[0] : fg[0]);
    g[4] = f2u(par ? fu[1] : fg[1]);
    const u32 old = lds_arrive(L + L_FLAG + F_GU, lane);
    if (old == 3) {
      const f32x4 a = *(const LAS f32x4*)(L + L_GU + lane * 4), c4 = *(const LAS f32x4*)(L + L_GU + 256 + lane * 4);
      st_pair(rs, WS_H + (gks * FF + gcg * 64 + lane) * 16, f2u((a[0] + a[1]) + (a[2] + a[3])), f2u((c4[0] + c4[1]) + (c4[2] + c4[3])), T0, T1);
      STAMP(8);
    }
  }
  if (cw == 0) {
    wait_long(t0 + TQ);
    v4u g[8];
    bool ok = false;
#pragma unroll 1
    for (u32 it = 0; it < SPIN_CAP; ++it) {
      if (!ok) {
#pragma unroll
        for (int k = 0; k < 8; ++k) g[k] = ld_sc1(rs, WS_Q + (512 * h + 64 * k + lane) * 16);
      }
      ok = true;
#pragma unroll
      for (int k = 0; k < 8; ++k) ok = ok && g[k][1] == T0 && g[k][3] == T1;
      if (__all(ok)) break;
      __builtin_amdgcn_s_sleep(16);
      asm volatile("" ::: "memory");
    }
#pragma unroll
    for (int k = 0; k < 2; ++k) {
      const float qa = (u2f(g[k][0]) + u2f(g[2 + k][0])) + (u2f(g[4 + k][0]) + u2f(g[6 + k][0]));
      const float qb = (u2f(g[k][2]) + u2f(g[2 + k][2])) + (u2f(g[4 + k][2]) + u2f(g[6 + k][2]));
      L[L_Q + 64 * k + lane] = cvtpk(qa, qb);
    }
    lds_signal(L + L_FLAG + F_QRDY, lane);
    STAMP(6);
    {
      const bool act = lane < 18;
      const int ho = WS_H + (18 * b + lane) * 16;
      v4u gh[GKS];
      ok = !act;
#pragma unroll
      for (int k = 0; k < GKS; ++k) gh[k] = v4u{0u, 0u, 0u, 0u};
#pragma unroll 1
      for (u32 it = 0; it < SPIN_CAP; ++it) {
        if (!ok) {
#pragma unroll
          for (int k = 0; k < GKS; ++k) gh[k] = ld_sc1(rs, ho + k * FF * 16);
          ok = true;
#pragma unroll
          for (int k = 0; k < GKS; ++k) ok = ok && gh[k][1] == T0 && gh[k][3] == T1;
        }
        if (__all(ok)) break;
        __builtin_amdgcn_s_sleep(16);
        asm volatile("" ::: "memory");
      }
      float gs = u2f(gh[0][0]), us = u2f(gh[0][2]);
#pragma unroll
      for (int k = 1; k < GKS; ++k) { gs += u2f(gh[k][0]); us += u2f(gh[k][2]); }
      const float gb = rbf(gs), ub = rbf(us);
      float hv = rbf(gb * rcp(1.f + __expf(-gb))) * ub;
      if (!act) hv = 0.f;
      if (lane < 24) L[L_HV + lane] = cvtpk(hv, hv);
      lds_signal(L + L_FLAG + F_H, lane);
      STAMP(9);
    }
    {
      wait_long(t0 + KV0 + (((255 + 3 * TPRE) * DLT) >> 4) - TWARM);
      const int off = WS_REC + (h * 16 + s) * 2048 + lane * 16;
      v4u r0 = {0u, 0u, 0u, 0u}, r1 = r0;
      bool ok0 = false, ok1 = false;
      const u32 T1h = T1 >> 8;
#pragma unroll 1
      for (u32 it = 0; it < SPIN_CAP; ++it) {
        if (!ok0) r0 = ld_sc1(rs, off);
        if (!ok1) r1 = ld_sc1(rs, off + 1024);
        ok0 = r0[1] == T0 && (r0[3] >> 8) == T1h;
        ok1 = r1[1] == T0 && (r1[3] >> 8) == T1h;
        if (__all(ok0 && ok1)) break;
        if (RDEL >= 0 && it == 0) { lds_wait_ge(L + L_FLAG + F_DONE, 8); wait_until(now() + RDEL); }
        asm volatile("" ::: "memory");
      }
      STAMP(10);
      const int sh = (lane & 3) * 8;
      u32 s0 = (r0[3] & 0xffu) << sh, s1 = (r1[3] & 0xffu) << sh;
      s0 |= dppu<XOR1>(s0); s1 |= dppu<XOR1>(s1);
      s0 |= dppu<XOR2>(s0); s1 |= dppu<XOR2>(s1);
      const u32 x0 = dppu<HMIR>(s0), x1 = dppu<HMIR>(s1);
      const bool up = lane & 4;
      const float m0 = u2f(up ? x0 : s0), l0 = u2f(up ? s0 : x0), m1 = u2f(up ? x1 : s1), l1 = u2f(up ? s1 : x1);
      float M = fmaxf(m0, m1);
      M = fmaxf(M, dpp<XOR8>(M));
      M = xrow_max(M);
      const float w0 = __expf(m0 - M), w1 = __expf(m1 - M);
      float ls = opq(w0 * l0) + opq(w1 * l1), oa = opq(w0 * u2f(r0[0])) + opq(w1 * u2f(r1[0])), ob = opq(w0 * u2f(r0[2])) + opq(w1 * u2f(r1[2]));
      ls += dpp<XOR8>(ls); oa += dpp<XOR8>(oa); ob += dpp<XOR8>(ob);
      ls = xrow_sum(ls); oa = xrow_sum(oa); ob = xrow_sum(ob);
      const float rl = rcp(ls);
      oa *= rl; ob *= rl;
      if (lane < 8) *(LAS v2u*)(L + L_OV + 2 * lane) = v2u{cvtpk(oa, oa), cvtpk(ob, ob)};
      lds_signal(L + L_FLAG + F_O, lane);
    }
    FLUSH();
    return;
  }
  {
    const int ct0 = 3 * (cw - 1);
    const rsrc_t rd = mkrs(wd_, FF * D * 2), ro = mkrs(wo_, QD * D * 2);
    const int dof = (18 * b + (lane >> 3)) * (D * 2) + ct0 * 128 + (lane & 7) * 16;
    const int dof2 = lane < 16 ? dof : OOB;
    const int oof = (256 * h + 16 * s + (lane >> 3)) * (D * 2) + ct0 * 128 + (lane & 7) * 16;
    v4u Wd[9], Wo[6];
    const u32 tt = t0 + KV0 + (((cw - 1) * DLT) >> 4);
#pragma unroll
    for (int i = 0; i < 15; ++i) {
      if (i == 0 || i == TPRE) wait_long(tt + ((((i < TPRE ? 8 * VGAP : 256) + 3 * i) * DLT) >> 4));
      else wait_until(tt + ((((i < TPRE ? 8 * VGAP : 256) + 3 * i) * DLT) >> 4));
      if (i == TPRE && cw == 1) STAMP(7);
      if (i < 9) Wd[i] = ld_nt(rd, i < 6 ? dof : dof2, (i / 3) * 8 * D * 2 + (i % 3) * 128);
      else Wo[i - 9] = ld_nt(ro, oof, ((i - 9) / 3) * 8 * D * 2 + (i % 3) * 128);
      SCHEDB();
    }
    lds_wait_ge(L + L_FLAG + F_H, 1);
    v4u Ah[3];
#pragma unroll
    for (int j = 0; j < 3; ++j) Ah[j] = L[L_HV + 8 * j + (lane >> 3)] & mk;
    f32x4 acc[3];
#pragma unroll
    for (int t = 0; t < 3; ++t) {
      acc[t] = mfma(Ah[0], Wd[t], f32x4{0.f, 0.f, 0.f, 0.f});
      acc[t] = mfma(Ah[1], Wd[3 + t], acc[t]);
      acc[t] = mfma(Ah[2], Wd[6 + t], acc[t]);
    }
    if (cw == 1) STAMP(17);
    vmwait<0>();
    if (cw == 1) STAMP(18);
    lds_wait_ge(L + L_FLAG + F_O, 1);
    const v4u A0 = L[L_OV + (lane >> 3)] & mk, A1 = L[L_OV + 8 + (lane >> 3)] & mk;
#pragma unroll
    for (int t = 0; t < 3; ++t) acc[t] = mfma(A0, Wo[t], acc[t]);
#pragma unroll
    for (int t = 0; t < 3; ++t) acc[t] = mfma(A1, Wo[3 + t], acc[t]);
    const int po = WS_PART + ((4 * ct0 + 2 * ((lane >> 4) & 1) + (lane >> 5)) * NWG + b) * 128 + (lane & 7) * 16;
    const f32x2 f0 = fold<0>(acc[0]), f1 = fold<1>(acc[1]), f2 = fold<0>(acc[2]);
    st_pair(rs, po + par * (4 * NWG * 128), f2u(par ? f1[0] : f0[0]), f2u(par ? f1[1] : f0[1]), T0, T1);
    if (!par) st_pair(rs, po + 2 * (4 * NWG * 128), f2u(f2[0]), f2u(f2[1]), T0, T1);
    lds_signal(L + L_FLAG + F_PUB, lane);
    if (cw == 1) STAMP(11);
  }
  FLUSH();
}

extern "C" void kernel_launch(void* const* d_in, const int* in_sizes, int n_in, void* d_out, int out_size, void* d_ws, size_t ws_size,
                              hipStream_t stream) {
  fused_kernel<<<NWG, NTHR, 0, stream>>>((const u16*)d_in[0], (const u16*)d_in[1], (const u16*)d_in[2], (const u16*)d_in[3],
                                         (const u16*)d_in[4], (const u16*)d_in[6], (const u16*)d_in[7], (const u16*)d_in[8],
                                         (u16*)d_out, (unsigned char*)d_ws);
}
```

```cpp
#define XK 0
#include <hip/hip_runtime.h>
#include <stdint.h>

#define GKS 3
#define CPRIO 3
#define APRIO 2
#define DLT 64
#define DLG (DLT * 27 / 32)
#define GU0 20
#define KV0 (GU0 + 46 * DLG / 16)
#define TPRE 4
#define VGAP 24
#define PVLAG 3
#define TQ 340
#define TWARM 100
#define MAPX 0
#define RDEL 80
#define DBG 0

#define GAS __attribute__((address_space(1)))
#define LAS __attribute__((address_space(3)))
typedef unsigned u32;
typedef unsigned long long u64;
typedef unsigned short u16;
typedef u32 v4u __attribute__((ext_vector_type(4)));
typedef u32 v2u __attribute__((ext_vector_type(2)));
typedef float f32x4 __attribute__((ext_vector_type(4)));
typedef float f32x2 __attribute__((ext_vector_type(2)));
typedef __bf16 bf16x8 __attribute__((ext_vector_type(8)));
typedef __bf16 bf16x2 __attribute__((ext_vector_type(2)));
typedef __amdgpu_buffer_rsrc_t rsrc_t;

constexpr int D = 576, SK = 8192, DH = 128, FF = 4608, QD = 4096;
constexpr int NWG = 256, NTHR = 768;
constexpr int NRED = 36, RED0 = NWG - NRED;
constexpr int NGU = 72 * GKS;
constexpr int GG = 18 / GKS;
constexpr int WS_EPOCH = 0;
constexpr int WS_Q = 4096;
constexpr int WS_REC = WS_Q + 16 * 4 * 128 * 16;
constexpr int WS_H = WS_REC + NWG * 16 * 8 * 16;
constexpr int WS_PART = WS_H + GKS * FF * 16;
constexpr int WS_END = WS_PART + NRED * NWG * 128;
constexpr int WS_DBG = 4 << 20;
constexpr u32 SPIN_CAP = 1u << 15;
constexpr int OOB = 0x40000000;
constexpr int L_FLAG = 0;
constexpr int L_TAG = 16;
constexpr int L_Q = 32;
constexpr int L_MX = L_Q + 128;
constexpr int L_LS = L_MX + 16;
constexpr int L_ORED = L_LS + 16;
constexpr int L_XT = L_ORED + 8 * 256;
constexpr int L_QP = L_XT + 640;
constexpr int L_GU = L_QP + 512;
constexpr int L_FIN = L_GU + 512;
constexpr int L_HV = L_FIN + 128;
constexpr int L_OV = L_HV + 32;
constexpr int L_WORDS = L_OV + 16;
enum { F_QRDY = 0, F_TRIG = 1, F_MAXC = 2, F_DONE = 3, F_GU = 4, F_PUB = 5, F_FIN = 6, F_XN = 7, F_QP = 8, F_LS = 9, F_H = 10, F_O = 11 };

__device__ __forceinline__ float u2f(u32 x) { return __builtin_bit_cast(float, x); }
__device__ __forceinline__ u32 f2u(float x) { return __builtin_bit_cast(u32, x); }
__device__ __forceinline__ float bf_lo(u32 x) { return u2f(x << 16); }
__device__ __forceinline__ float bf_hi(u32 x) { return u2f(x & 0xffff0000u); }
__device__ __forceinline__ u32 cvtpk(float lo, float hi) {
  f32x2 v = {lo, hi};
  return __builtin_bit_cast(u32, __builtin_convertvector(v, bf16x2));
}
__device__ __forceinline__ float rbf(float x) { return (float)(__bf16)x; }
__device__ __forceinline__ float rcp(float x) { return __builtin_amdgcn_rcpf(x); }
__device__ __forceinline__ float opq(float x) { asm volatile("" : "+v"(x)); return x; }
template <int CTRL> __device__ __forceinline__ u32 dppu(u32 x) { return (u32)__builtin_amdgcn_mov_dpp((int)x, CTRL, 0xf, 0xf, true); }
template <int CTRL> __device__ __forceinline__ float dpp(float x) { return u2f(dppu<CTRL>(f2u(x))); }
constexpr int XOR1 = 0xB1, XOR2 = 0x4E, HMIR = 0x141, XOR8 = 0x128;
__device__ __forceinline__ float xrow_max(float x) {
  auto s = __builtin_amdgcn_permlane16_swap(f2u(x), f2u(x), false, false);
  x = fmaxf(u2f(s[0]), u2f(s[1]));
  auto t = __builtin_amdgcn_permlane32_swap(f2u(x), f2u(x), false, false);
  return fmaxf(u2f(t[0]), u2f(t[1]));
}
__device__ __forceinline__ float xrow_sum(float x) {
  auto s = __builtin_amdgcn_permlane16_swap(f2u(x), f2u(x), false, false);
  x = u2f(s[0]) + u2f(s[1]);
  auto t = __builtin_amdgcn_permlane32_swap(f2u(x), f2u(x), false, false);
  return u2f(t[0]) + u2f(t[1]);
}
__device__ __forceinline__ float row_sum(float x) {
  x = opq(x);
  x += dpp<XOR1>(x); x += dpp<XOR2>(x); x += dpp<HMIR>(x); x += dpp<XOR8>(x); return x;
}
__device__ __forceinline__ float row_max(float x) {
  x = fmaxf(x, dpp<XOR1>(x)); x = fmaxf(x, dpp<XOR2>(x)); x = fmaxf(x, dpp<HMIR>(x)); x = fmaxf(x, dpp<XOR8>(x)); return x;
}
__device__ __forceinline__ f32x4 mfma(v4u a, v4u b, f32x4 c) {
  return __builtin_amdgcn_mfma_f32_16x16x32_bf16(__builtin_bit_cast(bf16x8, a), __builtin_bit_cast(bf16x8, b), c, 0, 0, 0);
}
template <int ODD> __device__ __forceinline__ f32x2 fold(f32x4 a) {
  auto s0 = __builtin_amdgcn_permlane32_swap(f2u(a[0]), f2u(a[2]), false, false);
  auto s1 = __builtin_amdgcn_permlane32_swap(f2u(a[1]), f2u(a[3]), false, false);
  f32x2 r;
  if (ODD) { r[0] = dpp<XOR8>(u2f(s0[0])) + u2f(s0[1]); r[1] = dpp<XOR8>(u2f(s1[0])) + u2f(s1[1]); }
  else { r[0] = u2f(s0[0]) + dpp<XOR8>(u2f(s0[1])); r[1] = u2f(s1[0]) + dpp<XOR8>(u2f(s1[1])); }
  return r;
}
#define LGKM0() asm volatile("s_waitcnt lgkmcnt(0)" ::: "memory")
#define SCHEDB() __builtin_amdgcn_sched_barrier(0)
template <int N> __device__ __forceinline__ void vmwait() { asm volatile("s_waitcnt vmcnt(%0)" ::"i"(N) : "memory"); }
__device__ __forceinline__ u32 lds_ld(LAS u32* p) { return __hip_atomic_load(p, __ATOMIC_RELAXED, __HIP_MEMORY_SCOPE_WORKGROUP); }
__device__ __forceinline__ void lds_wait_ge(LAS u32* p, u32 n) {
#pragma unroll 1
  for (u32 i = 0; i < (1u << 20); ++i) {
    if ((u32)__builtin_amdgcn_readfirstlane(lds_ld(p)) >= n) break;
    __builtin_amdgcn_s_sleep(1);
  }
  asm volatile("" ::: "memory");
}
__device__ __forceinline__ u32 lds_arrive(LAS u32* p, int lane) {
  LGKM0();
  u32 r = 0;
  if (lane == 0) r = __hip_atomic_fetch_add(p, 1u, __ATOMIC_RELAXED, __HIP_MEMORY_SCOPE_WORKGROUP);
  r = __builtin_amdgcn_readfirstlane(r);
  asm volatile("" ::: "memory");
  return r;
}
__device__ __forceinline__ void lds_signal(LAS u32* p, int lane) {
  LGKM0();
  if (lane == 0) __hip_atomic_fetch_add(p, 1u, __ATOMIC_RELAXED, __HIP_MEMORY_SCOPE_WORKGROUP);
  asm volatile("" ::: "memory");
}
__device__ __forceinline__ u32 now() { return (u32)__builtin_amdgcn_s_memrealtime(); }
template <int SL> __device__ __forceinline__ void wait_sl(u32 t) {
  asm volatile("s_branch .Lwt%=\n"
               ".Lws%=:\n\ts_sleep %1\n"
               ".Lwt%=:\n\ts_memrealtime vcc\n\ts_waitcnt lgkmcnt(0)\n\ts_sub_i32 vcc_hi, %0, vcc_lo\n\ts_cmp_gt_i32 vcc_hi, 0\n\ts_cbranch_scc1 .Lws%="
               : : "s"(t), "i"(SL) : "memory", "scc", "vcc");
}
__device__ __forceinline__ void wait_until(u32 t) { wait_sl<1>(t); }
__device__ __forceinline__ void wait_long(u32 t) { wait_sl<12>(t - 45); wait_sl<1>(t); }
template <int I> struct IC { static constexpr int value = I; };
template <int I, int N, class F> __device__ __forceinline__ void static_for(F&& f) {
  if constexpr (I < N) { f(IC<I>{}); static_for<I + 1, N>(f); }
}
constexpr int QK0 = 16 + PVLAG - 1;
constexpr int pv_done(int i) {
  if (i < QK0 + 6) return 0;
  if (i >= 32) return 16;
  const int p = pv_done(i - 1), a = i - 16 - PVLAG + 1;
  return a < p ? p : (a < p + 3 ? a : p + 3);
}
constexpr int AUX_SC1 = 16, AUX_NT = 2;
__device__ __forceinline__ rsrc_t mkrs(const void* p, int bytes) { return __builtin_amdgcn_make_buffer_rsrc((void*)p, 0, bytes, 0x00020000); }
__device__ __forceinline__ v4u ld_nt(rsrc_t r, int voff, int soff) { return __builtin_amdgcn_raw_buffer_load_b128(r, voff, soff, AUX_NT); }
__device__ __forceinline__ v4u ld_sc1(rsrc_t rs, int off) { return __builtin_amdgcn_raw_buffer_load_b128(rs, off, 0, AUX_SC1); }
__device__ __forceinline__ void st_pair(rsrc_t rs, int off, u32 a, u32 b, u32 T0, u32 T1) {
  v4u v = {a, T0, b, T1};
  __builtin_amdgcn_raw_buffer_store_b128(v, rs, off, 0, AUX_SC1);
}
#define STAMP(k) do { if ((k) == XK && blockIdx.x == 128) { tx = now(); hx = true; } } while (0)
#define FLUSH() do { if (hx) wait_long(tx + 3000); } while (0)

__global__ void __launch_bounds__(NTHR, 3)
fused_kernel(const u16* __restrict__ x_, const u16* __restrict__ kc_, const u16* __restrict__ vc_, const u16* __restrict__ wq_,
             const u16* __restrict__ wo_, const u16* __restrict__ wg_, const u16* __restrict__ wu_, const u16* __restrict__ wd_,
             u16* __restrict__ out_, unsigned char* ws_) {
  __shared__ __attribute__((aligned(16))) u32 lds_raw[L_WORDS];
  LAS u32* L = (LAS u32*)lds_raw;
  asm volatile("" ::"s"(x_), "s"(kc_), "s"(vc_), "s"(wq_), "s"(wo_), "s"(wg_), "s"(wu_), "s"(wd_), "s"(out_), "s"(ws_));
  const u32 t0 = now();
  const int b = blockIdx.x;
  const int lane = threadIdx.x & 63;
  const int wave = __builtin_amdgcn_readfirstlane(threadIdx.x >> 6);
  const int h = (b & 7) * 2 + ((b >> 3) & 1), s = b >> 4;
  const int cw = wave - 8;
  const rsrc_t rs = mkrs(ws_, WS_END);
  u32 tx = 0; bool hx = false;
  v4u mk;
  {
    const int e = lane & 7;
    const u32 hv = (e & 1) ? 0xffff0000u : 0x0000ffffu;
    mk[0] = (e >> 1) == 0 ? hv : 0u; mk[1] = (e >> 1) == 1 ? hv : 0u; mk[2] = (e >> 1) == 2 ? hv : 0u; mk[3] = (e >> 1) == 3 ? hv : 0u;
  }
  const int par = (lane >> 3) & 1;
  LAS u32* xt = L + L_XT;

  if (__builtin_expect(wave < 8, 1)) {
    if (wave == 0) STAMP(0);
    v4u K[16], V[16];
    const size_t base = ((size_t)(h * SK + s * 512) * DH) * 2;
    const rsrc_t rk = mkrs((const char*)kc_ + base, 512 * DH * 2), rv = mkrs((const char*)vc_ + base, 512 * DH * 2);
    const int ko = wave * 1024 + (((lane >> 2) & 3) * 16 + (lane >> 4) * 4 + (lane & 3)) * 16;
    const int vo = wave * 1024 + lane * 16;
    const u32 tb = t0 + KV0 + ((wave * DLT) >> 4);
    const int hb = par;
    const bool t1 = (lane >> 2) & 1;
    v4u Be, Bo;
    float sc[8];
    u32 pk[8], tw[2], T0 = 0, T1 = 0;
    float m = -INFINITY;
    f32x4 acc0 = {0.f, 0.f, 0.f, 0.f}, acc1 = {0.f, 0.f, 0.f, 0.f};
    __builtin_amdgcn_s_setprio(APRIO);
    __builtin_amdgcn_s_barrier();
    static_for<0, 33>([&](auto ic) __attribute__((always_inline)) {
      constexpr int i = decltype(ic)::value;
      if constexpr (i < 32) {
        if constexpr (i == 0) wait_long(tb);
        else wait_until(tb + ((i * DLT) >> 1) + (i >= VGAP ? (3 * TPRE * DLT) >> 4 : 0));
        if constexpr (i < 16) K[i] = ld_nt(rk, ko, i * 8192); else V[i - 16] = ld_nt(rv, vo, (i - 16) * 8192);
        SCHEDB();
      }
      if (i == 31 && wave == 7) STAMP(1);
      if constexpr (i == QK0) {
        lds_wait_ge(L + L_FLAG + F_QRDY, 1);
        if (wave == 0) STAMP(2);
        const v4u qf = *(const LAS v4u*)(L + L_Q + hb * 64 + (lane >> 4) * 16 + (lane & 3) * 4);
        const v4u zero4 = {0u, 0u, 0u, 0u};
        Be = t1 ? zero4 : qf; Bo = t1 ? qf : zero4;
      }
      if constexpr (i >= QK0 && i < QK0 + 4) {
#pragma unroll
        for (int j = 2 * (i - QK0); j < 2 * (i - QK0) + 2; ++j) {
          f32x4 d = {0.f, 0.f, 0.f, 0.f};
          d = mfma(K[2 * j], Be, d);
          d = mfma(K[2 * j + 1], Bo, d);
          float v = (dpp<0x00>(d[0]) + dpp<0x55>(d[1])) + (dpp<0xAA>(d[2]) + dpp<0xFF>(d[3]));
          v = rbf(rbf(v) * 0.08838834764831845f);
          sc[j] = v;
          m = fmaxf(m, v);
        }
      }
      if constexpr (i == QK0 + 3) {
        m = fmaxf(m, dpp<HMIR>(m));
        m = xrow_max(m);
        if ((lane & 55) == 0) L[L_MX + hb * 8 + wave] = f2u(m);
        lds_signal(L + L_FLAG + F_MAXC, lane);
      }
      if constexpr (i == QK0 + 5) {
        lds_wait_ge(L + L_FLAG + F_MAXC, 8);
        const f32x4 a = *(const LAS f32x4*)(L + L_MX + hb * 8), c = *(const LAS f32x4*)(L + L_MX + hb * 8 + 4);
        const float M = fmaxf(fmaxf(fmaxf(a[0], a[1]), fmaxf(a[2], a[3])), fmaxf(fmaxf(c[0], c[1]), fmaxf(c[2], c[3])));
        float ls = 0.f;
#pragma unroll
        for (int j = 0; j < 8; ++j) {
          const float p = __expf(sc[j] - M);
          ls += p;
          pk[j] = cvtpk(p, p);
        }
        ls += dpp<HMIR>(ls);
        ls = xrow_sum(ls);
        if ((lane & 55) == 0) L[L_LS + hb * 8 + wave] = f2u(ls);
        lds_signal(L + L_FLAG + F_LS, lane);
        if (wave == 0) STAMP(3);
      }
      if constexpr (i == QK0 + 8) {
        lds_wait_ge(L + L_FLAG + F_LS, 8);
        const v2u tg = *(const LAS v2u*)(L + L_TAG);
        T0 = __builtin_amdgcn_readfirstlane(tg[0]); T1 = __builtin_amdgcn_readfirstlane(tg[1]);
#pragma unroll
        for (int hh = 0; hh < 2; ++hh) {
          const LAS u32* sp = L + ((lane & 4) ? L_LS : L_MX) + hh * 8;
          const f32x4 a = *(const LAS f32x4*)sp, c = *(const LAS f32x4*)(sp + 4);
          const float Mh = fmaxf(fmaxf(fmaxf(a[0], a[1]), fmaxf(a[2], a[3])), fmaxf(fmaxf(c[0], c[1]), fmaxf(c[2], c[3])));
          const float Lh = ((a[0] + a[1]) + (a[2] + a[3])) + ((c[0] + c[1]) + (c[2] + c[3]));
          tw[hh] = (T1 & 0xffffff00u) | ((f2u((lane & 4) ? Lh : Mh) >> ((lane & 3) * 8)) & 0xffu);
        }
      }
      if constexpr (i >= QK0 + 6) {
        constexpr int lo = pv_done(i - 1), hi = pv_done(i);
#pragma unroll
        for (int j = lo; j < hi; ++j) {
          const u32 pp = pk[j >> 1], pm = dppu<HMIR>(pp);
          if (j & 1) acc1 = mfma(v4u{pm & mk[0], pm & mk[1], pp & mk[2], pp & mk[3]}, V[j], acc1);
          else acc0 = mfma(v4u{pp & mk[0], pp & mk[1], pm & mk[2], pm & mk[3]}, V[j], acc0);
        }
      }
      SCHEDB();
    });
    acc0 += acc1;
    *(LAS f32x4*)(L + L_ORED + wave * 256 + (lane >> 5) * 128 + 8 * (lane & 15) + 4 * ((lane >> 4) & 1)) = acc0;
    if (wave == 7) STAMP(15);
    const u32 old = lds_arrive(L + L_FLAG + F_DONE, lane);
    if (old == 7) {
      f32x2 r[2][8];
#pragma unroll
      for (int k = 0; k < 8; ++k) {
        r[0][k] = *(const LAS f32x2*)(L + L_ORED + k * 256 + 2 * lane);
        r[1][k] = *(const LAS f32x2*)(L + L_ORED + k * 256 + 128 + 2 * lane);
      }
#pragma unroll
      for (int hh = 0; hh < 2; ++hh) {
        const f32x2 o = ((r[hh][0] + r[hh][1]) + (r[hh][2] + r[hh][3])) + ((r[hh][4] + r[hh][5]) + (r[hh][6] + r[hh][7]));
        const int rec = (h * 16 + hh * 8 + (lane >> 3)) * 16 + s;
        st_pair(rs, WS_REC + (rec * 8 + (lane & 7)) * 16, f2u(o[0]), f2u(o[1]), T0, tw[hh]);
      }
      STAMP(4);
    }
    if (b >= RED0) {
      const int r = b - RED0;
      const int col = 64 * (r >> 2) + 4 * ((r >> 1) & 1) + 2 * (r & 1) + 8 * ((lane >> 1) & 7) + (lane & 1);
      const u32 xo = x_[col];
      const int pb = WS_PART + (r * NWG + wave * 32) * 128 + lane * 16;
      v4u g[4];
      bool ok[4];
#pragma unroll
      for (int k = 0; k < 4; ++k) { g[k] = v4u{0u, 0u, 0u, 0u}; ok[k] = false; }
#pragma unroll 1
      for (u32 it = 0; it < SPIN_CAP; ++it) {
#pragma unroll
        for (int k = 0; k < 4; ++k)
          if (!ok[k]) g[k] = ld_sc1(rs, pb + k * 1024);
        bool all = true;
#pragma unroll
        for (int k = 0; k < 4; ++k) { ok[k] = ok[k] || (g[k][1] == T0 && g[k][3] == T1); all = all && ok[k]; }
        if (__all(all)) break;
        if (it == 0) { lds_wait_ge(L + L_FLAG + F_PUB, 3); if (wave == 0) STAMP(12); }
        asm volatile("" ::: "memory");
      }
      float a0 = (u2f(g[0][0]) + u2f(g[1][0])) + (u2f(g[2][0]) + u2f(g[3][0]));
      float a1 = (u2f(g[0][2]) + u2f(g[1][2])) + (u2f(g[2][2]) + u2f(g[3][2]));
      a0 += dpp<XOR8>(a0); a1 += dpp<XOR8>(a1);
      a0 = xrow_sum(a0); a1 = xrow_sum(a1);
      if (lane < 8) *(LAS v2u*)(L + L_FIN + wave * 16 + 2 * lane) = v2u{f2u(a0), f2u(a1)};
      const u32 old2 = lds_arrive(L + L_FLAG + F_FIN, lane);
      if (old2 == 7) {
        if (lane < 16) {
          const LAS float* f = (const LAS float*)(L + L_FIN) + lane;
          const float A = ((f[0] + f[16]) + (f[32] + f[48])) + ((f[64] + f[80]) + (f[96] + f[112]));
          const float o = bf_lo(xo) + A;
          out_[col] = (u16)cvtpk(o, o);
        }
        if (r == 0 && lane == 0) {
          const v2u e1 = {T0, T1 - T0 * 0x9E3779B1u};
          __builtin_amdgcn_raw_buffer_store_b64(e1, rs, WS_EPOCH, 0, AUX_SC1);
        }
        STAMP(13);
      }
    }
    FLUSH();
    return;
  }

  __builtin_amdgcn_s_setprio(CPRIO);
  const bool hasgu = b < NGU;
  const int gcg = b / GKS, gks = b % GKS;
  const int grp0 = (72 / GKS) * gks + GG * cw;
  const rsrc_t rg = mkrs(wg_, D * FF * 2), ru = mkrs(wu_, D * FF * 2);
  const int go = (grp0 * 8 + (lane >> 3)) * (FF * 2) + gcg * 128 + (lane & 7) * 16 + (hasgu ? 0 : OOB);
  v4u Wg[GG], Wu[GG];
  u32 T0, T1;
  const u32 tg0 = t0 + GU0 + (((cw ? cw - 1 : 6 * GG) * DLG) >> 4);
  const int gstep = cw ? 3 * DLG : DLG;
#define GULOADS() do { _Pragma("unroll") for (int i = 0; i < GG; ++i) { \
    wait_until(tg0 + ((2 * i * gstep) >> 4)); Wg[i] = ld_nt(rg, go, i * 8 * FF * 2); SCHEDB(); \
    wait_until(tg0 + (((2 * i + 1) * gstep) >> 4)); Wu[i] = ld_nt(ru, go, i * 8 * FF * 2); SCHEDB(); } } while (0)
  if (cw == 0) {
    const v4u xa = *((const GAS v4u*)x_ + lane);
    const v4u xb = *((const GAS v4u*)x_ + 64 + (lane & 7));
    const u64 ep = __hip_atomic_load((GAS u64*)(ws_ + WS_EPOCH), __ATOMIC_RELAXED, __HIP_MEMORY_SCOPE_AGENT);
    SCHEDB();
    if (lane < 16) L[L_FLAG + lane] = 0u;
    LGKM0();
    __builtin_amdgcn_s_barrier();
    float v[16];
#pragma unroll
    for (int k = 0; k < 4; ++k) { v[2 * k] = bf_lo(xa[k]); v[2 * k + 1] = bf_hi(xa[k]); v[8 + 2 * k] = bf_lo(xb[k]); v[9 + 2 * k] = bf_hi(xb[k]); }
    const bool tail = lane < 8;
    float s0 = ((v[0] + v[1]) + (v[2] + v[3])) + ((v[4] + v[5]) + (v[6] + v[7]));
    float s1 = ((v[8] + v[9]) + (v[10] + v[11])) + ((v[12] + v[13]) + (v[14] + v[15]));
    const float mean = xrow_sum(row_sum(s0 + (tail ? s1 : 0.f))) * (1.f / D);
    float q0 = 0.f, q1 = 0.f;
#pragma unroll
    for (int k = 0; k < 8; ++k) { float d0 = v[k] - mean, d1 = v[8 + k] - mean; q0 += d0 * d0; q1 += d1 * d1; }
    const float var = xrow_sum(row_sum(q0 + (tail ? q1 : 0.f))) * (1.f / D);
    const float mb = rbf(mean), vb = rbf(var);
    const float rden = rcp(rbf(__builtin_amdgcn_sqrtf(rbf(vb + 1e-5f))));
#pragma unroll
    for (int k = 0; k < 8; ++k) {
      const float a = rbf(v[k] - mb) * rden;
      xt[k * 80 + lane] = cvtpk(a, a);
    }
    if (tail) {
#pragma unroll
      for (int k = 0; k < 8; ++k) {
        const float a = rbf(v[8 + k] - mb) * rden;
        xt[k * 80 + 64 + lane] = cvtpk(a, a);
      }
    }
    const u64 e1 = ep + 1;
    T0 = __builtin_amdgcn_readfirstlane((u32)e1);
    T1 = __builtin_amdgcn_readfirstlane((u32)(e1 >> 32)) + T0 * 0x9E3779B1u;
    if (lane == 0) *(LAS v2u*)(L + L_TAG) = v2u{T0, T1};
    lds_signal(L + L_FLAG + F_XN, lane);
    STAMP(14);
    GULOADS();
  } else {
    const int r0 = 144 * (s & 3) + 48 * (cw - 1);
    const rsrc_t rq = mkrs(wq_, D * QD * 2);
    const int qo = (r0 + (lane >> 3)) * (QD * 2) + (256 * h + 64 * (s >> 2)) * 2 + (lane & 7) * 16;
    v4u Wq[6];
#pragma unroll
    for (int i = 0; i < 6; ++i) Wq[i] = ld_nt(rq, qo, i * 8 * QD * 2);
    SCHEDB();
    __builtin_amdgcn_s_barrier();
    GULOADS();
    lds_wait_ge(L + L_FLAG + F_XN, 1);
    const LAS u32* xr = xt + (lane >> 3) * 80 + (r0 >> 3);
    const v2u tg = *(const LAS v2u*)(L + L_TAG);
    f32x4 a0 = {0.f, 0.f, 0.f, 0.f}, a1 = {0.f, 0.f, 0.f, 0.f};
#pragma unroll
    for (int i = 0; i < 6; i += 2) {
      const v2u xv = *(const LAS v2u*)(xr + i);
      a0 = mfma(xv[0] & mk, Wq[i], a0);
      a1 = mfma(xv[1] & mk, Wq[i + 1], a1);
    }
    a0 += a1;
    T0 = __builtin_amdgcn_readfirstlane(tg[0]);
    T1 = __builtin_amdgcn_readfirstlane(tg[1]);
    if ((lane >> 5) == par) {
      LAS u32* g = L + L_QP + (8 * (lane & 7) + 4 * ((lane >> 4) & 1)) * 8 + (cw - 1) * 2 + par;
#pragma unroll
      for (int r = 0; r < 4; ++r) g[r * 8] = f2u(a0[r]);
    }
    const u32 old = lds_arrive(L + L_FLAG + F_QP, lane);
    if (old == 2) {
      const LAS u32* qp = L + L_QP + (lane & 31) * 16;
      const f32x4 a = *(const LAS f32x4*)qp, a2 = *(const LAS f32x4*)(qp + 8);
      const f32x2 c = *(const LAS f32x2*)(qp + 4), c2 = *(const LAS f32x2*)(qp + 12);
      const float q = ((a[0] + a[1]) + (a[2] + a[3])) + (c[0] + c[1]);
      const float qn = ((a2[0] + a2[1]) + (a2[2] + a2[3])) + (c2[0] + c2[1]);
      if (lane < 32) st_pair(rs, WS_Q + ((h * 4 + (s & 3)) * 128 + 32 * (s >> 2) + lane) * 16, f2u(q), f2u(qn), T0, T1);
      STAMP(5);
    }
  }
  if (hasgu) {
    const LAS u32* xr = xt + (lane >> 3) * 80 + grp0;
    f32x4 ag = {0.f, 0.f, 0.f, 0.f}, au = {0.f, 0.f, 0.f, 0.f};
#pragma unroll
    for (int i = 0; i < GG; ++i) {
      const v4u A = xr[i] & mk;
      ag = mfma(A, Wg[i], ag);
      au = mfma(A, Wu[i], au);
    }
    const f32x2 fg = fold<0>(ag), fu = fold<1>(au);
    const int c = 8 * (lane & 7) + 4 * ((lane >> 4) & 1) + 2 * (lane >> 5);
    LAS u32* g = L + L_GU + (par * 64 + c) * 4 + cw;
    g[0] = f2u(par ? fu[0] : fg[0]);
    g[4] = f2u(par ? fu[1] : fg[1]);
    const u32 old = lds_arrive(L + L_FLAG + F_GU, lane);
    if (old == 3) {
      const f32x4 a = *(const LAS f32x4*)(L + L_GU + lane * 4), c4 = *(const LAS f32x4*)(L + L_GU + 256 + lane * 4);
      st_pair(rs, WS_H + (gks * FF + gcg * 64 + lane) * 16, f2u((a[0] + a[1]) + (a[2] + a[3])), f2u((c4[0] + c4[1]) + (c4[2] + c4[3])), T0, T1);
      STAMP(8);
    }
  }
  if (cw == 0) {
    wait_long(t0 + TQ);
    v4u g[8];
    bool ok = false;
#pragma unroll 1
    for (u32 it = 0; it < SPIN_CAP; ++it) {
      if (!ok) {
#pragma unroll
        for (int k = 0; k < 8; ++k) g[k] = ld_sc1(rs, WS_Q + (512 * h + 64 * k + lane) * 16);
      }
      ok = true;
#pragma unroll
      for (int k = 0; k < 8; ++k) ok = ok && g[k][1] == T0 && g[k][3] == T1;
      if (__all(ok)) break;
      __builtin_amdgcn_s_sleep(16);
      asm volatile("" ::: "memory");
    }
#pragma unroll
    for (int k = 0; k < 2; ++k) {
      const float qa = (u2f(g[k][0]) + u2f(g[2 + k][0])) + (u2f(g[4 + k][0]) + u2f(g[6 + k][0]));
      const float qb = (u2f(g[k][2]) + u2f(g[2 + k][2])) + (u2f(g[4 + k][2]) + u2f(g[6 + k][2]));
      L[L_Q + 64 * k + lane] = cvtpk(qa, qb);
    }
    lds_signal(L + L_FLAG + F_QRDY, lane);
    STAMP(6);
    {
      const bool act = lane < 18;
      const int ho = WS_H + (18 * b + lane) * 16;
      v4u gh[GKS];
      ok = !act;
#pragma unroll
      for (int k = 0; k < GKS; ++k) gh[k] = v4u{0u, 0u, 0u, 0u};
#pragma unroll 1
      for (u32 it = 0; it < SPIN_CAP; ++it) {
        if (!ok) {
#pragma unroll
          for (int k = 0; k < GKS; ++k) gh[k] = ld_sc1(rs, ho + k * FF * 16);
          ok = true;
#pragma unroll
          for (int k = 0; k < GKS; ++k) ok = ok && gh[k][1] == T0 && gh[k][3] == T1;
        }
        if (__all(ok)) break;
        __builtin_amdgcn_s_sleep(16);
        asm volatile("" ::: "memory");
      }
      float gs = u2f(gh[0][0]), us = u2f(gh[0][2]);
#pragma unroll
      for (int k = 1; k < GKS; ++k) { gs += u2f(gh[k][0]); us += u2f(gh[k][2]); }
      const float gb = rbf(gs), ub = rbf(us);
      float hv = rbf(gb * rcp(1.f + __expf(-gb))) * ub;
      if (!act) hv = 0.f;
      if (lane < 24) L[L_HV + lane] = cvtpk(hv, hv);
      lds_signal(L + L_FLAG + F_H, lane);
      STAMP(9);
    }
    {
      wait_long(t0 + KV0 + (((255 + 3 * TPRE) * DLT) >> 4) - TWARM);
      const int off = WS_REC + (h * 16 + s) * 2048 + lane * 16;
      v4u r0 = {0u, 0u, 0u, 0u}, r1 = r0;
      bool ok0 = false, ok1 = false;
      const u32 T1h = T1 >> 8;
#pragma unroll 1
      for (u32 it = 0; it < SPIN_CAP; ++it) {
        if (!ok0) r0 = ld_sc1(rs, off);
        if (!ok1) r1 = ld_sc1(rs, off + 1024);
        ok0 = r0[1] == T0 && (r0[3] >> 8) == T1h;
        ok1 = r1[1] == T0 && (r1[3] >> 8) == T1h;
        if (__all(ok0 && ok1)) break;
        if (RDEL >= 0 && it == 0) { lds_wait_ge(L + L_FLAG + F_DONE, 8); wait_until(now() + RDEL); }
        asm volatile("" ::: "memory");
      }
      STAMP(10);
      const int sh = (lane & 3) * 8;
      u32 s0 = (r0[3] & 0xffu) << sh, s1 = (r1[3] & 0xffu) << sh;
      s0 |= dppu<XOR1>(s0); s1 |= dppu<XOR1>(s1);
      s0 |= dppu<XOR2>(s0); s1 |= dppu<XOR2>(s1);
      const u32 x0 = dppu<HMIR>(s0), x1 = dppu<HMIR>(s1);
      const bool up = lane & 4;
      const float m0 = u2f(up ? x0 : s0), l0 = u2f(up ? s0 : x0), m1 = u2f(up ? x1 : s1), l1 = u2f(up ? s1 : x1);
      float M = fmaxf(m0, m1);
      M = fmaxf(M, dpp<XOR8>(M));
      M = xrow_max(M);
      const float w0 = __expf(m0 - M), w1 = __expf(m1 - M);
      float ls = opq(w0 * l0) + opq(w1 * l1), oa = opq(w0 * u2f(r0[0])) + opq(w1 * u2f(r1[0])), ob = opq(w0 * u2f(r0[2])) + opq(w1 * u2f(r1[2]));
      ls += dpp<XOR8>(ls); oa += dpp<XOR8>(oa); ob += dpp<XOR8>(ob);
      ls = xrow_sum(ls); oa = xrow_sum(oa); ob = xrow_sum(ob);
      const float rl = rcp(ls);
      oa *= rl; ob *= rl;
      if (lane < 8) *(LAS v2u*)(L + L_OV + 2 * lane) = v2u{cvtpk(oa, oa), cvtpk(ob, ob)};
      lds_signal(L + L_FLAG + F_O, lane);
    }
    FLUSH();
    return;
  }
  {
    const int ct0 = 3 * (cw - 1);
    const rsrc_t rd = mkrs(wd_, FF * D * 2), ro = mkrs(wo_, QD * D * 2);
    const int dof = (18 * b + (lane >> 3)) * (D * 2) + ct0 * 128 + (lane & 7) * 16;
    const int dof2 = lane < 16 ? dof : OOB;
    const int oof = (256 * h + 16 * s + (lane >> 3)) * (D * 2) + ct0 * 128 + (lane & 7) * 16;
    v4u Wd[9], Wo[6];
    const u32 tt = t0 + KV0 + (((cw - 1) * DLT) >> 4);
#pragma unroll
    for (int i = 0; i < 15; ++i) {
      if (i == 0 || i == TPRE) wait_long(tt + ((((i < TPRE ? 8 * VGAP : 256) + 3 * i) * DLT) >> 4));
      else wait_until(tt + ((((i < TPRE ? 8 * VGAP : 256) + 3 * i) * DLT) >> 4));
      if (i == TPRE && cw == 1) STAMP(7);
      if (i < 9) Wd[i] = ld_nt(rd, i < 6 ? dof : dof2, (i / 3) * 8 * D * 2 + (i % 3) * 128);
      else Wo[i - 9] = ld_nt(ro, oof, ((i - 9) / 3) * 8 * D * 2 + (i % 3) * 128);
      SCHEDB();
    }
    lds_wait_ge(L + L_FLAG + F_H, 1);
    v4u Ah[3];
#pragma unroll
    for (int j = 0; j < 3; ++j) Ah[j] = L[L_HV + 8 * j + (lane >> 3)] & mk;
    f32x4 acc[3];
#pragma unroll
    for (int t = 0; t < 3; ++t) {
      acc[t] = mfma(Ah[0], Wd[t], f32x4{0.f, 0.f, 0.f, 0.f});
      acc[t] = mfma(Ah[1], Wd[3 + t], acc[t]);
      acc[t] = mfma(Ah[2], Wd[6 + t], acc[t]);
    }
    if (cw == 1) STAMP(17);
    vmwait<0>();
    if (cw == 1) STAMP(18);
    lds_wait_ge(L + L_FLAG + F_O, 1);
    const v4u A0 = L[L_OV + (lane >> 3)] & mk, A1 = L[L_OV + 8 + (lane >> 3)] & mk;
#pragma unroll
    for (int t = 0; t < 3; ++t) acc[t] = mfma(A0, Wo[t], acc[t]);
#pragma unroll
    for (int t = 0; t < 3; ++t) acc[t] = mfma(A1, Wo[3 + t], acc[t]);
    const int po = WS_PART + ((4 * ct0 + 2 * ((lane >> 4) & 1) + (lane >> 5)) * NWG + b) * 128 + (lane & 7) * 16;
    const f32x2 f0 = fold<0>(acc[0]), f1 = fold<1>(acc[1]), f2 = fold<0>(acc[2]);
    st_pair(rs, po + par * (4 * NWG * 128), f2u(par ? f1[0] : f0[0]), f2u(par ? f1[1] : f0[1]), T0, T1);
    if (!par) st_pair(rs, po + 2 * (4 * NWG * 128), f2u(f2[0]), f2u(f2[1]), T0, T1);
    lds_signal(L + L_FLAG + F_PUB, lane);
    if (cw == 1) STAMP(11);
  }
  FLUSH();
}

extern "C" void kernel_launch(void* const* d_in, const int* in_sizes, int n_in, void* d_out, int out_size, void* d_ws, size_t ws_size,
                              hipStream_t stream) {
  fused_kernel<<<NWG, NTHR, 0, stream>>>((const u16*)d_in[0], (const u16*)d_in[1], (const u16*)d_in[2], (const u16*)d_in[3],
                                         (const u16*)d_in[4], (const u16*)d_in[6], (const u16*)d_in[7], (const u16*)d_in[8],
                                         (u16*)d_out, (unsigned char*)d_ws);
}
```

```cpp
#include <hip/hip_runtime.h>
#include <stdint.h>

#define CPRIO 3
#define APRIO 2
#define DLT 62
#define KV0 30
#define QK0 20
#define PVLAG 3
#define PVMAX 4
#define WIN 4
#define G0 2
#define TPRE 4
#define VGAP 24
#define TH 900
#define TQ 450
#define RDEL 60
#define PDEL 40
#define DBG 0

#define GAS __attribute__((address_space(1)))
#define LAS __attribute__((address_space(3)))
typedef unsigned u32;
typedef unsigned long long u64;
typedef unsigned short u16;
typedef u32 v4u __attribute__((ext_vector_type(4)));
typedef u32 v3u __attribute__((ext_vector_type(3)));
typedef u32 v2u __attribute__((ext_vector_type(2)));
typedef float f32x4 __attribute__((ext_vector_type(4)));
typedef float f32x2 __attribute__((ext_vector_type(2)));
typedef __bf16 bf16x8 __attribute__((ext_vector_type(8)));
typedef __bf16 bf16x2 __attribute__((ext_vector_type(2)));
typedef __amdgpu_buffer_rsrc_t rsrc_t;

constexpr int D = 576, SK = 8192, DH = 128, FF = 4608, QD = 4096;
constexpr int NWG = 256, NTHR = 768;
constexpr int NRED = 36, RED0 = NWG - NRED;
constexpr int GKS = 3, NGU = 72 * GKS;
constexpr int WS_EPOCH = 0;
constexpr int WS_Q = 4096;
constexpr int WS_REC = WS_Q + 16 * 4 * 128 * 16;
constexpr int WS_H = WS_REC + NWG * 16 * 8 * 16;
constexpr int WS_PART = WS_H + GKS * FF * 16;
constexpr int WS_END = WS_PART + NRED * NWG * 128;
constexpr int WS_DBG = 4 << 20;
constexpr u32 SPIN_CAP = 1u << 15;
constexpr int OOB = 0x40000000;
constexpr int L_FLAG = 0;
constexpr int L_TAG = 16;
constexpr int L_Q = 32;
constexpr int L_MX = L_Q + 128;
constexpr int L_LS = L_MX + 16;
constexpr int L_ORED = L_LS + 16;
constexpr int L_XT = L_ORED + 8 * 256;
constexpr int L_QP = L_XT + 640;
constexpr int L_GU = L_QP + 512;
constexpr int L_FIN = L_GU + 512;
constexpr int L_HV = L_FIN + 128;
constexpr int L_OV = L_HV + 32;
constexpr int L_WORDS = L_OV + 16;
enum { F_QRDY = 0, F_MAXC = 2, F_DONE = 3, F_GU = 4, F_PUB = 5, F_FIN = 6, F_XN = 7, F_QP = 8, F_LS = 9, F_H = 10, F_O = 11 };

__device__ __forceinline__ float u2f(u32 x) { return __builtin_bit_cast(float, x); }
__device__ __forceinline__ u32 f2u(float x) { return __builtin_bit_cast(u32, x); }
__device__ __forceinline__ float bf_lo(u32 x) { return u2f(x << 16); }
__device__ __forceinline__ float bf_hi(u32 x) { return u2f(x & 0xffff0000u); }
__device__ __forceinline__ u32 cvtpk(float lo, float hi) {
  f32x2 v = {lo, hi};
  return __builtin_bit_cast(u32, __builtin_convertvector(v, bf16x2));
}
__device__ __forceinline__ float rbf(float x) { return (float)(__bf16)x; }
__device__ __forceinline__ float rcp(float x) { return __builtin_amdgcn_rcpf(x); }
__device__ __forceinline__ float opq(float x) { asm volatile("" : "+v"(x)); return x; }
template <int CTRL> __device__ __forceinline__ u32 dppu(u32 x) { return (u32)__builtin_amdgcn_mov_dpp((int)x, CTRL, 0xf, 0xf, true); }
template <int CTRL> __device__ __forceinline__ float dpp(float x) { return u2f(dppu<CTRL>(f2u(x))); }
constexpr int XOR1 = 0xB1, XOR2 = 0x4E, HMIR = 0x141, XOR8 = 0x128;
__device__ __forceinline__ float xrow_max(float x) {
  auto s = __builtin_amdgcn_permlane16_swap(f2u(x), f2u(x), false, false);
  x = fmaxf(u2f(s[0]), u2f(s[1]));
  auto t = __builtin_amdgcn_permlane32_swap(f2u(x), f2u(x), false, false);
  return fmaxf(u2f(t[0]), u2f(t[1]));
}
__device__ __forceinline__ float xrow_sum(float x) {
  auto s = __builtin_amdgcn_permlane16_swap(f2u(x), f2u(x), false, false);
  x = u2f(s[0]) + u2f(s[1]);
  auto t = __builtin_amdgcn_permlane32_swap(f2u(x), f2u(x), false, false);
  return u2f(t[0]) + u2f(t[1]);
}
__device__ __forceinline__ float row_sum(float x) {
  x = opq(x);
  x += dpp<XOR1>(x); x += dpp<XOR2>(x); x += dpp<HMIR>(x); x += dpp<XOR8>(x); return x;
}
__device__ __forceinline__ float row_max(float x) {
  x = fmaxf(x, dpp<XOR1>(x)); x = fmaxf(x, dpp<XOR2>(x)); x = fmaxf(x, dpp<HMIR>(x)); x = fmaxf(x, dpp<XOR8>(x)); return x;
}
__device__ __forceinline__ f32x4 mfma(v4u a, v4u b, f32x4 c) {
  return __builtin_amdgcn_mfma_f32_16x16x32_bf16(__builtin_bit_cast(bf16x8, a), __builtin_bit_cast(bf16x8, b), c, 0, 0, 0);
}
template <int ODD> __device__ __forceinline__ f32x2 fold(f32x4 a) {
  auto s0 = __builtin_amdgcn_permlane32_swap(f2u(a[0]), f2u(a[2]), false, false);
  auto s1 = __builtin_amdgcn_permlane32_swap(f2u(a[1]), f2u(a[3]), false, false);
  f32x2 r;
  if (ODD) { r[0] = dpp<XOR8>(u2f(s0[0])) + u2f(s0[1]); r[1] = dpp<XOR8>(u2f(s1[0])) + u2f(s1[1]); }
  else { r[0] = u2f(s0[0]) + dpp<XOR8>(u2f(s0[1])); r[1] = u2f(s1[0]) + dpp<XOR8>(u2f(s1[1])); }
  return r;
}
#define LGKM0() asm volatile("s_waitcnt lgkmcnt(0)" ::: "memory")
#define SCHEDB() __builtin_amdgcn_sched_barrier(0)
template <int N> __device__ __forceinline__ void vmwait() { asm volatile("s_waitcnt vmcnt(%0)" ::"i"(N) : "memory"); }
__device__ __forceinline__ u32 lds_ld(LAS u32* p) { return __hip_atomic_load(p, __ATOMIC_RELAXED, __HIP_MEMORY_SCOPE_WORKGROUP); }
__device__ __forceinline__ void lds_wait_ge(LAS u32* p, u32 n) {
#pragma unroll 1
  for (u32 i = 0; i < (1u << 20); ++i) {
    if ((u32)__builtin_amdgcn_readfirstlane(lds_ld(p)) >= n) break;
    __builtin_amdgcn_s_sleep(1);
  }
  asm volatile("" ::: "memory");
}
__device__ __forceinline__ u32 lds_arrive(LAS u32* p, int lane) {
  LGKM0();
  u32 r = 0;
  if (lane == 0) r = __hip_atomic_fetch_add(p, 1u, __ATOMIC_RELAXED, __HIP_MEMORY_SCOPE_WORKGROUP);
  r = __builtin_amdgcn_readfirstlane(r);
  asm volatile("" ::: "memory");
  return r;
}
__device__ __forceinline__ void lds_signal(LAS u32* p, int lane) {
  LGKM0();
  if (lane == 0) __hip_atomic_fetch_add(p, 1u, __ATOMIC_RELAXED, __HIP_MEMORY_SCOPE_WORKGROUP);
  asm volatile("" ::: "memory");
}
__device__ __forceinline__ u32 now() { return (u32)__builtin_amdgcn_s_memrealtime(); }
template <int SL> __device__ __forceinline__ void wait_sl(u32 t) {
  asm volatile("s_branch .Lwt%=\n"
               ".Lws%=:\n\ts_sleep %1\n"
               ".Lwt%=:\n\ts_memrealtime vcc\n\ts_waitcnt lgkmcnt(0)\n\ts_sub_i32 vcc_hi, %0, vcc_lo\n\ts_cmp_gt_i32 vcc_hi, 0\n\ts_cbranch_scc1 .Lws%="
               : : "s"(t), "i"(SL) : "memory", "scc", "vcc");
}
__device__ __forceinline__ void wait_until(u32 t) { wait_sl<1>(t); }
__device__ __forceinline__ void wait_long(u32 t) { wait_sl<12>(t - 45); wait_sl<1>(t); }
template <int I> struct IC { static constexpr int value = I; };
template <int I, int N, class F> __device__ __forceinline__ void static_for(F&& f) {
  if constexpr (I < N) { f(IC<I>{}); static_for<I + 1, N>(f); }
}
constexpr int pv_done(int i) {
  if (i < QK0 + 6) return 0;
  if (i >= 32) return 16;
  const int p = pv_done(i - 1), a = i - 16 - PVLAG + 1;
  return a < p ? p : (a < p + PVMAX ? a : p + PVMAX);
}
__device__ __forceinline__ float dot2(u32 a, u32 b, float c) {
  return __builtin_amdgcn_fdot2_f32_bf16(__builtin_bit_cast(bf16x2, a), __builtin_bit_cast(bf16x2, b), c, false);
}
constexpr int SL16(int n16) { return (n16 * DLT) >> 8; }
constexpr int GSL = 3 * 16 * 27 / 32;
constexpr int tail_pos(int i) { return 3 * (i < 6 ? 16 * i : i < 9 ? 96 + 4 * (i - 6) : 108 + 16 * (i - 9)); }
constexpr int kv_pos(int i) {
  const int g = i - G0 < 0 ? 0 : (i - G0 > 16 ? 16 : i - G0);
  return 128 * i + GSL * g + (i >= VGAP ? tail_pos(TPRE) : 0);
}
constexpr int AUX_SC1 = 16, AUX_NT = 2;
__device__ __forceinline__ rsrc_t mkrs(const void* p, int bytes) { return __builtin_amdgcn_make_buffer_rsrc((void*)p, 0, bytes, 0x00020000); }
__device__ __forceinline__ v4u ld_nt(rsrc_t r, int voff, int soff) { return __builtin_amdgcn_raw_buffer_load_b128(r, voff, soff, AUX_NT); }
__device__ __forceinline__ v4u ld_sc1(rsrc_t rs, int off) { return __builtin_amdgcn_raw_buffer_load_b128(rs, off, 0, AUX_SC1); }
__device__ __forceinline__ void st_pair(rsrc_t rs, int off, u32 a, u32 b, u32 T0, u32 T1) {
  v4u v = {a, T0, b, T1};
  __builtin_amdgcn_raw_buffer_store_b128(v, rs, off, 0, AUX_SC1);
}
#define STAMP(k) do { } while (0)
#define FLUSH() do { } while (0)

__global__ void __launch_bounds__(NTHR, 3) __attribute__((aligned(4096)))
fused_kernel(const u16* __restrict__ x_, const u16* __restrict__ kc_, const u16* __restrict__ vc_, const u16* __restrict__ wq_,
             const u16* __restrict__ wo_, const u16* __restrict__ wg_, const u16* __restrict__ wu_, const u16* __restrict__ wd_,
             u16* __restrict__ out_, unsigned char* ws_) {
  __shared__ __attribute__((aligned(16))) u32 lds_raw[L_WORDS];
  LAS u32* L = (LAS u32*)lds_raw;
  u32 cpf;
  {
    u64 pc; u32 len;
    asm volatile("s_getpc_b64 %0\n.Lfk_pc:\n\ts_mov_b32 %1, .Lfk_e0-.Lfk_pc\n\ts_max_i32 %1, %1, .Lfk_e1-.Lfk_pc\n\ts_max_i32 %1, %1, .Lfk_e2-.Lfk_pc"
                 : "=s"(pc), "=s"(len) : : "scc");
    cpf = __builtin_amdgcn_raw_buffer_load_b32(__builtin_amdgcn_make_buffer_rsrc((void*)pc, 0, len, 0x00020000), threadIdx.x * 64, 0, 0);
    SCHEDB();
  }
  const int b = blockIdx.x;
  const int lane = threadIdx.x & 63;
  const int wave = __builtin_amdgcn_readfirstlane(threadIdx.x >> 6);
  const int h = (b & 7) * 2 + ((b >> 3) & 1), s = b >> 4;
  const int cw = wave - 8;
  asm volatile("" ::"s"(x_), "s"(kc_), "s"(vc_), "s"(wq_), "s"(wo_), "s"(wg_), "s"(wu_), "s"(wd_), "s"(out_), "s"(ws_));
  const rsrc_t rs = mkrs(ws_, WS_END);
  v4u W0[6], xa, xb;
  v2u ep;
  const size_t kvbase = ((size_t)(h * SK + s * 512) * DH) * 2;
  const rsrc_t rk = mkrs((const char*)kc_ + kvbase, 512 * DH * 2), rv = mkrs((const char*)vc_ + kvbase, 512 * DH * 2);
  const int ko = wave * 1024 + (((lane >> 2) & 3) * 16 + (lane >> 4) * 4 + (lane & 3)) * 16;
  const int r0 = 144 * (s & 3) + 48 * (cw - 1);
  {
    const rsrc_t rq = mkrs(wave < 8 ? (const char*)kc_ + kvbase : (const char*)wq_, wave < 8 ? 512 * DH * 2 : wave == 8 ? 0 : D * QD * 2);
    const int qo = (r0 + (lane >> 3)) * (QD * 2) + (256 * h + 64 * (s >> 2)) * 2 + (lane & 7) * 16;
    W0[0] = ld_nt(rq, wave < 8 ? ko : qo, 0);
    if (wave >= 8) {
      const rsrc_t rx = mkrs(x_, wave == 8 ? D * 2 : 0), re = mkrs(ws_, wave == 8 ? 8 : 0);
      xa = __builtin_amdgcn_raw_buffer_load_b128(rx, lane * 16, 0, 0);
      xb = __builtin_amdgcn_raw_buffer_load_b128(rx, 1024 + (lane & 7) * 16, 0, 0);
      ep = __builtin_amdgcn_raw_buffer_load_b64(re, WS_EPOCH, 0, AUX_SC1);
#pragma unroll
      for (int i = 1; i < 6; ++i) W0[i] = ld_nt(rq, qo, i * 8 * QD * 2);
    }
  }
  SCHEDB();
  const u32 t0 = now();
  if (wave == 8) {
    if (lane < 16) L[L_FLAG + lane] = 0u;
    LGKM0();
  }
  v4u mk;
  {
    const int e = lane & 7;
    const u32 hv = (e & 1) ? 0xffff0000u : 0x0000ffffu;
    mk[0] = (e >> 1) == 0 ? hv : 0u; mk[1] = (e >> 1) == 1 ? hv : 0u; mk[2] = (e >> 1) == 2 ? hv : 0u; mk[3] = (e >> 1) == 3 ? hv : 0u;
  }
  const int par = (lane >> 3) & 1;
  LAS u32* xt = L + L_XT;
  __builtin_amdgcn_s_barrier();
  u32 T0 = 0, T1 = 0;

  if (wave == 8) {
    __builtin_amdgcn_s_setprio(CPRIO);
    float v[16];
#pragma unroll
    for (int k = 0; k < 4; ++k) { v[2 * k] = bf_lo(xa[k]); v[2 * k + 1] = bf_hi(xa[k]); v[8 + 2 * k] = bf_lo(xb[k]); v[9 + 2 * k] = bf_hi(xb[k]); }
    const bool tail = lane < 8;
    float s0 = ((v[0] + v[1]) + (v[2] + v[3])) + ((v[4] + v[5]) + (v[6] + v[7]));
    float s1 = ((v[8] + v[9]) + (v[10] + v[11])) + ((v[12] + v[13]) + (v[14] + v[15]));
    const float mean = xrow_sum(row_sum(s0 + (tail ? s1 : 0.f))) * (1.f / D);
    float q0 = 0.f, q1 = 0.f;
#pragma unroll
    for (int k = 0; k < 8; ++k) { float d0 = v[k] - mean, d1 = v[8 + k] - mean; q0 += d0 * d0; q1 += d1 * d1; }
    const float var = xrow_sum(row_sum(q0 + (tail ? q1 : 0.f))) * (1.f / D);
    const float mb = rbf(mean), vb = rbf(var);
    const float rden = rcp(rbf(__builtin_amdgcn_sqrtf(rbf(vb + 1e-5f))));
#pragma unroll
    for (int k = 0; k < 8; ++k) {
      const float a = rbf(v[k] - mb) * rden;
      xt[k * 80 + lane] = cvtpk(a, a);
    }
    if (tail) {
#pragma unroll
      for (int k = 0; k < 8; ++k) {
        const float a = rbf(v[8 + k] - mb) * rden;
        xt[k * 80 + 64 + lane] = cvtpk(a, a);
      }
    }
    const u64 e1 = (((u64)(u32)__builtin_amdgcn_readfirstlane(ep[1]) << 32) | (u32)__builtin_amdgcn_readfirstlane(ep[0])) + 1;
    T0 = (u32)e1;
    T1 = (u32)(e1 >> 32) + T0 * 0x9E3779B1u;
    if (lane == 0) *(LAS v2u*)(L + L_TAG) = v2u{T0, T1};
    lds_signal(L + L_FLAG + F_XN, lane);
    STAMP(14);
  } else if (wave > 8) {
    __builtin_amdgcn_s_setprio(CPRIO);
    lds_wait_ge(L + L_FLAG + F_XN, 1);
    const LAS u32* xr = xt + (lane >> 3) * 80 + (r0 >> 3);
    const v2u tg = *(const LAS v2u*)(L + L_TAG);
    f32x4 a0 = {0.f, 0.f, 0.f, 0.f}, a1 = {0.f, 0.f, 0.f, 0.f};
#pragma unroll
    for (int i = 0; i < 6; i += 2) {
      const v2u xv = *(const LAS v2u*)(xr + i);
      a0 = mfma(xv[0] & mk, W0[i], a0);
      a1 = mfma(xv[1] & mk, W0[i + 1], a1);
    }
    a0 += a1;
    T0 = __builtin_amdgcn_readfirstlane(tg[0]);
    T1 = __builtin_amdgcn_readfirstlane(tg[1]);
    if ((lane >> 5) == par) {
      LAS u32* g = L + L_QP + (8 * (lane & 7) + 4 * ((lane >> 4) & 1)) * 8 + (cw - 1) * 2 + par;
#pragma unroll
      for (int r = 0; r < 4; ++r) g[r * 8] = f2u(a0[r]);
    }
    const u32 old = lds_arrive(L + L_FLAG + F_QP, lane);
    if (old == 2) {
      const LAS u32* qp = L + L_QP + (lane & 31) * 16;
      const f32x4 a = *(const LAS f32x4*)qp, a2 = *(const LAS f32x4*)(qp + 8);
      const f32x2 c = *(const LAS f32x2*)(qp + 4), c2 = *(const LAS f32x2*)(qp + 12);
      const float q = ((a[0] + a[1]) + (a[2] + a[3])) + (c[0] + c[1]);
      const float qn = ((a2[0] + a2[1]) + (a2[2] + a2[3])) + (c2[0] + c2[1]);
      if (lane < 32) st_pair(rs, WS_Q + ((h * 4 + (s & 3)) * 128 + 32 * (s >> 2) + lane) * 16, f2u(q), f2u(qn), T0, T1);
      STAMP(5);
    }
  }

  if (wave < 8) {
    if (wave == 0) STAMP(0);
    v4u K[16], V[16];
    K[0] = W0[0];
    const int vo = wave * 1024 + lane * 16;
    const u32 tb = t0 + KV0 + ((wave * DLT) >> 4);
    const int hb = par;
    const bool t1 = (lane >> 2) & 1;
    v4u Be, Bo;
    float sc[8];
    u32 pk[8], tw[2];
    float m = -INFINITY;
    f32x4 acc0 = {0.f, 0.f, 0.f, 0.f}, acc1 = {0.f, 0.f, 0.f, 0.f};
    __builtin_amdgcn_s_setprio(APRIO);
    static_for<1, 33>([&](auto ic) __attribute__((always_inline)) {
      constexpr int i = decltype(ic)::value;
      if constexpr (i < 32) {
        wait_until(tb + SL16(kv_pos(i)));
        if constexpr (i > WIN) vmwait<WIN>();
        if constexpr (i == 1) V[0] = ld_nt(rv, vo, 0);
        else if constexpr (i < 17) K[i - 1] = ld_nt(rk, ko, (i - 1) * 8192);
        else V[i - 16] = ld_nt(rv, vo, (i - 16) * 8192);
        SCHEDB();
      }
      if constexpr (i == WIN + 1) asm volatile("" ::"v"(cpf));
      if (i == 31 && wave == 7) STAMP(1);
      if constexpr (i == QK0) {
        lds_wait_ge(L + L_FLAG + F_QRDY, 1);
        if (wave == 0) STAMP(2);
        const v4u qf = *(const LAS v4u*)(L + L_Q + hb * 64 + (lane >> 4) * 16 + (lane & 3) * 4);
        const v4u zero4 = {0u, 0u, 0u, 0u};
        Be = t1 ? zero4 : qf; Bo = t1 ? qf : zero4;
      }
      if constexpr (i >= QK0 && i < QK0 + 4) {
#pragma unroll
        for (int j = 2 * (i - QK0); j < 2 * (i - QK0) + 2; ++j) {
          f32x4 d = {0.f, 0.f, 0.f, 0.f};
          d = mfma(K[2 * j], Be, d);
          d = mfma(K[2 * j + 1], Bo, d);
          float v = (dpp<0x00>(d[0]) + dpp<0x55>(d[1])) + (dpp<0xAA>(d[2]) + dpp<0xFF>(d[3]));
          v = rbf(rbf(v) * 0.08838834764831845f);
          sc[j] = v;
          m = fmaxf(m, v);
        }
      }
      if constexpr (i == QK0 + 3) {
        m = fmaxf(m, dpp<HMIR>(m));
        m = xrow_max(m);
        if ((lane & 55) == 0) L[L_MX + hb * 8 + wave] = f2u(m);
        lds_signal(L + L_FLAG + F_MAXC, lane);
      }
      if constexpr (i == QK0 + 5) {
        lds_wait_ge(L + L_FLAG + F_MAXC, 8);
        const f32x4 a = *(const LAS f32x4*)(L + L_MX + hb * 8), c = *(const LAS f32x4*)(L + L_MX + hb * 8 + 4);
        const float M = fmaxf(fmaxf(fmaxf(a[0], a[1]), fmaxf(a[2], a[3])), fmaxf(fmaxf(c[0], c[1]), fmaxf(c[2], c[3])));
        float ls = 0.f;
#pragma unroll
        for (int j = 0; j < 8; ++j) {
          const float p = __expf(sc[j] - M);
          ls += p;
          pk[j] = cvtpk(p, p);
        }
        ls += dpp<HMIR>(ls);
        ls = xrow_sum(ls);
        if ((lane & 55) == 0) L[L_LS + hb * 8 + wave] = f2u(ls);
        lds_signal(L + L_FLAG + F_LS, lane);
        if (wave == 0) STAMP(3);
      }
      if constexpr (i == QK0 + 8) {
        lds_wait_ge(L + L_FLAG + F_LS, 8);
        const v2u tg = *(const LAS v2u*)(L + L_TAG);
        T0 = __builtin_amdgcn_readfirstlane(tg[0]); T1 = __builtin_amdgcn_readfirstlane(tg[1]);
#pragma unroll
        for (int hh = 0; hh < 2; ++hh) {
          const LAS u32* sp = L + ((lane & 4) ? L_LS : L_MX) + hh * 8;
          const f32x4 a = *(const LAS f32x4*)sp, c = *(const LAS f32x4*)(sp + 4);
          const float Mh = fmaxf(fmaxf(fmaxf(a[0], a[1]), fmaxf(a[2], a[3])), fmaxf(fmaxf(c[0], c[1]), fmaxf(c[2], c[3])));
          const float Lh = ((a[0] + a[1]) + (a[2] + a[3])) + ((c[0] + c[1]) + (c[2] + c[3]));
          tw[hh] = (T1 & 0xffffff00u) | ((f2u((lane & 4) ? Lh : Mh) >> ((lane & 3) * 8)) & 0xffu);
        }
      }
      if constexpr (i >= QK0 + 6) {
        constexpr int lo = pv_done(i - 1), hi = pv_done(i);
#pragma unroll
        for (int j = lo; j < hi; ++j) {
          const u32 pp = pk[j >> 1], pm = dppu<HMIR>(pp);
          if (j & 1) acc1 = mfma(v4u{pm & mk[0], pm & mk[1], pp & mk[2], pp & mk[3]}, V[j], acc1);
          else acc0 = mfma(v4u{pp & mk[0], pp & mk[1], pm & mk[2], pm & mk[3]}, V[j], acc0);
        }
      }
      SCHEDB();
    });
    acc0 += acc1;
    *(LAS f32x4*)(L + L_ORED + wave * 256 + (lane >> 5) * 128 + 8 * (lane & 15) + 4 * ((lane >> 4) & 1)) = acc0;
    if (wave == 7) STAMP(15);
    const u32 old = lds_arrive(L + L_FLAG + F_DONE, lane);
    if (old == 7) {
      f32x2 r[2][8];
#pragma unroll
      for (int k = 0; k < 8; ++k) {
        r[0][k] = *(const LAS f32x2*)(L + L_ORED + k * 256 + 2 * lane);
        r[1][k] = *(const LAS f32x2*)(L + L_ORED + k * 256 + 128 + 2 * lane);
      }
#pragma unroll
      for (int hh = 0; hh < 2; ++hh) {
        const f32x2 o = ((r[hh][0] + r[hh][1]) + (r[hh][2] + r[hh][3])) + ((r[hh][4] + r[hh][5]) + (r[hh][6] + r[hh][7]));
        const int rec = (h * 16 + hh * 8 + (lane >> 3)) * 16 + s;
        st_pair(rs, WS_REC + (rec * 8 + (lane & 7)) * 16, f2u(o[0]), f2u(o[1]), T0, tw[hh]);
      }
      STAMP(4);
    }
    if (b >= RED0) {
      const int r = b - RED0;
      const int col = 64 * (r >> 2) + 4 * ((r >> 1) & 1) + 2 * (r & 1) + 8 * ((lane >> 1) & 7) + (lane & 1);
      const u32 xo = x_[col];
      const int pb = WS_PART + (r * NWG + wave * 32) * 128 + lane * 16;
      v4u g[4];
      bool ok[4];
#pragma unroll
      for (int k = 0; k < 4; ++k) { g[k] = v4u{0u, 0u, 0u, 0u}; ok[k] = false; }
      lds_wait_ge(L + L_FLAG + F_PUB, 3);
      wait_until(now() + PDEL);
      if (wave == 0) STAMP(12);
#pragma unroll 1
      for (u32 it = 0; it < SPIN_CAP; ++it) {
#pragma unroll
        for (int k = 0; k < 4; ++k)
          if (!ok[k]) g[k] = ld_sc1(rs, pb + k * 1024);
        bool all = true;
#pragma unroll
        for (int k = 0; k < 4; ++k) { ok[k] = ok[k] || (g[k][1] == T0 && g[k][3] == T1); all = all && ok[k]; }
        if (__all(all)) break;
        asm volatile("" ::: "memory");
      }
      float a0 = (u2f(g[0][0]) + u2f(g[1][0])) + (u2f(g[2][0]) + u2f(g[3][0]));
      float a1 = (u2f(g[0][2]) + u2f(g[1][2])) + (u2f(g[2][2]) + u2f(g[3][2]));
      a0 += dpp<XOR8>(a0); a1 += dpp<XOR8>(a1);
      a0 = xrow_sum(a0); a1 = xrow_sum(a1);
      if (lane < 8) *(LAS v2u*)(L + L_FIN + wave * 16 + 2 * lane) = v2u{f2u(a0), f2u(a1)};
      const u32 old2 = lds_arrive(L + L_FLAG + F_FIN, lane);
      if (old2 == 7) {
        if (lane < 16) {
          const LAS float* f = (const LAS float*)(L + L_FIN) + lane;
          const float A = ((f[0] + f[16]) + (f[32] + f[48])) + ((f[64] + f[80]) + (f[96] + f[112]));
          const float o = bf_lo(xo) + A;
          out_[col] = (u16)cvtpk(o, o);
        }
        if (r == 0 && lane == 0) {
          const v2u e1 = {T0, T1 - T0 * 0x9E3779B1u};
          __builtin_amdgcn_raw_buffer_store_b64(e1, rs, WS_EPOCH, 0, AUX_SC1);
        }
        asm volatile(".Lfk_e0:");
        STAMP(13);
      }
    }
    FLUSH();
    return;
  }

  asm volatile("" ::"v"(cpf));
  if (wave == 8) {
    u32 tch;
    {
      const char* p = (lane & 1) ? (const char*)wd_ + (18 * b) * (D * 2) : (const char*)wo_ + (256 * h + 16 * s) * (D * 2);
      if (lane & 2) p = (const char*)out_;
      tch = *(const GAS u32*)p;
    }
    wait_long(t0 + TQ);
    asm volatile("" ::"v"(tch));
    v4u g[8];
    bool ok = false;
#pragma unroll 1
    for (u32 it = 0; it < SPIN_CAP; ++it) {
      if (!ok) {
#pragma unroll
        for (int k = 0; k < 8; ++k) g[k] = ld_sc1(rs, WS_Q + (512 * h + 64 * k + lane) * 16);
      }
      ok = true;
#pragma unroll
      for (int k = 0; k < 8; ++k) ok = ok && g[k][1] == T0 && g[k][3] == T1;
      if (__all(ok)) break;
      __builtin_amdgcn_s_sleep(16);
      asm volatile("" ::: "memory");
    }
#pragma unroll
    for (int k = 0; k < 2; ++k) {
      const float qa = (u2f(g[k][0]) + u2f(g[2 + k][0])) + (u2f(g[4 + k][0]) + u2f(g[6 + k][0]));
      const float qb = (u2f(g[k][2]) + u2f(g[2 + k][2])) + (u2f(g[4 + k][2]) + u2f(g[6 + k][2]));
      L[L_Q + 64 * k + lane] = cvtpk(qa, qb);
    }
    lds_signal(L + L_FLAG + F_QRDY, lane);
    STAMP(6);
    {
      wait_long(t0 + TH);
      const bool act = lane < 18;
      const int ho = WS_H + (18 * b + lane) * 16;
      v4u gh[GKS];
      ok = !act;
#pragma unroll
      for (int k = 0; k < GKS; ++k) gh[k] = v4u{0u, 0u, 0u, 0u};
#pragma unroll 1
      for (u32 it = 0; it < SPIN_CAP; ++it) {
        if (!ok) {
#pragma unroll
          for (int k = 0; k < GKS; ++k) gh[k] = ld_sc1(rs, ho + k * FF * 16);
          ok = true;
#pragma unroll
          for (int k = 0; k < GKS; ++k) ok = ok && gh[k][1] == T0 && gh[k][3] == T1;
        }
        if (__all(ok)) break;
        __builtin_amdgcn_s_sleep(16);
        asm volatile("" ::: "memory");
      }
      float gs = u2f(gh[0][0]), us = u2f(gh[0][2]);
#pragma unroll
      for (int k = 1; k < GKS; ++k) { gs += u2f(gh[k][0]); us += u2f(gh[k][2]); }
      const float gb = rbf(gs), ub = rbf(us);
      float hv = rbf(gb * rcp(1.f + __expf(-gb))) * ub;
      if (!act) hv = 0.f;
      if (lane < 24) L[L_HV + lane] = cvtpk(hv, hv);
      lds_signal(L + L_FLAG + F_H, lane);
      STAMP(9);
    }
    {
      wait_long(t0 + KV0 + SL16(kv_pos(32)));
      lds_wait_ge(L + L_FLAG + F_DONE, 8);
      wait_until(now() + RDEL);
      const int off = WS_REC + (h * 16 + s) * 2048 + lane * 16;
      v4u r0 = {0u, 0u, 0u, 0u}, r1 = r0;
      bool ok0 = false, ok1 = false;
      const u32 T1h = T1 >> 8;
      STAMP(19);
#pragma unroll 1
      for (u32 it = 0; it < SPIN_CAP; ++it) {
        if (!ok0) r0 = ld_sc1(rs, off);
        if (!ok1) r1 = ld_sc1(rs, off + 1024);
        ok0 = r0[1] == T0 && (r0[3] >> 8) == T1h;
        ok1 = r1[1] == T0 && (r1[3] >> 8) == T1h;
        if (__all(ok0 && ok1)) break;
        __builtin_amdgcn_s_sleep(4);
        asm volatile("" ::: "memory");
      }
      STAMP(10);
      const int sh = (lane & 3) * 8;
      u32 s0 = (r0[3] & 0xffu) << sh, s1 = (r1[3] & 0xffu) << sh;
      s0 |= dppu<XOR1>(s0); s1 |= dppu<XOR1>(s1);
      s0 |= dppu<XOR2>(s0); s1 |= dppu<XOR2>(s1);
      const u32 x0 = dppu<HMIR>(s0), x1 = dppu<HMIR>(s1);
      const bool up = lane & 4;
      const float m0 = u2f(up ? x0 : s0), l0 = u2f(up ? s0 : x0), m1 = u2f(up ? x1 : s1), l1 = u2f(up ? s1 : x1);
      float M = fmaxf(m0, m1);
      M = fmaxf(M, dpp<XOR8>(M));
      M = xrow_max(M);
      const float w0 = __expf(m0 - M), w1 = __expf(m1 - M);
      float ls = opq(w0 * l0) + opq(w1 * l1), oa = opq(w0 * u2f(r0[0])) + opq(w1 * u2f(r1[0])), ob = opq(w0 * u2f(r0[2])) + opq(w1 * u2f(r1[2]));
      ls += dpp<XOR8>(ls); oa += dpp<XOR8>(oa); ob += dpp<XOR8>(ob);
      ls = xrow_sum(ls); oa = xrow_sum(oa); ob = xrow_sum(ob);
      const float rl = rcp(ls);
      oa *= rl; ob *= rl;
      if (lane < 8) *(LAS v2u*)(L + L_OV + 2 * lane) = v2u{cvtpk(oa, oa), cvtpk(ob, ob)};
      lds_signal(L + L_FLAG + F_O, lane);
      asm volatile(".Lfk_e1:");
    }
    FLUSH();
    return;
  }
  const int c = cw - 1;
  if (b < NGU) {
    const int gcg = b / GKS, gks = b % GKS;
    const int grp0 = 24 * gks + 8 * c;
    const rsrc_t rg = mkrs(wg_, D * FF * 2), ru = mkrs(wu_, D * FF * 2);
    const int go = (grp0 * 8 + (lane >> 3)) * (FF * 2) + gcg * 128 + (lane & 7) * 16;
    const u32 tg = t0 + KV0 + SL16(128 * G0) + c * SL16((128 + GSL) / 3);
    v4u Wg[16];
#pragma unroll
    for (int i = 0; i < 16; ++i) {
      wait_until(tg + SL16((128 + GSL) * i));
      Wg[i] = ld_nt((i & 1) ? ru : rg, go, (i >> 1) * 8 * FF * 2);
      SCHEDB();
    }
    const LAS u32* xr = xt + (lane >> 3) * 80 + grp0;
    f32x4 ag = {0.f, 0.f, 0.f, 0.f}, au = {0.f, 0.f, 0.f, 0.f};
#pragma unroll
    for (int i = 0; i < 8; ++i) {
      const v4u A = xr[i] & mk;
      ag = mfma(A, Wg[2 * i], ag);
      au = mfma(A, Wg[2 * i + 1], au);
    }
    const f32x2 fg = fold<0>(ag), fu = fold<1>(au);
    const int cc = 8 * (lane & 7) + 4 * ((lane >> 4) & 1) + 2 * (lane >> 5);
    LAS u32* g = L + L_GU + (par * 64 + cc) * 4 + c;
    g[0] = f2u(par ? fu[0] : fg[0]);
    g[4] = f2u(par ? fu[1] : fg[1]);
    const u32 old = lds_arrive(L + L_FLAG + F_GU, lane);
    if (old == 2) {
      const f32x4 a = *(const LAS f32x4*)(L + L_GU + lane * 4), c4 = *(const LAS f32x4*)(L + L_GU + 256 + lane * 4);
      st_pair(rs, WS_H + (gks * FF + gcg * 64 + lane) * 16, f2u((a[0] + a[1]) + a[2]), f2u((c4[0] + c4[1]) + c4[2]), T0, T1);
      STAMP(8);
    }
  }
  {
    const int ct0 = 3 * c;
    const rsrc_t rd = mkrs(wd_, FF * D * 2), ro = mkrs(wo_, QD * D * 2);
    const int dof = (18 * b + (lane >> 3)) * (D * 2) + ct0 * 128 + (lane & 7) * 16;
    const int dof2 = lane < 16 ? dof : OOB;
    const int oof = (256 * h + 16 * s + (lane >> 3)) * (D * 2) + ct0 * 128 + (lane & 7) * 16;
    v4u Wd[9], Wo[6], Ah[3];
    const u32 tt = t0 + KV0 + c * SL16(16);
#pragma unroll
    for (int i = 0; i < 15; ++i) {
      const u32 tw_ = tt + SL16((i < TPRE ? kv_pos(VGAP) : kv_pos(32)) - tail_pos(TPRE) + tail_pos(i));
      if (i == 0 || i == TPRE) wait_long(tw_); else wait_until(tw_);
      if (i == TPRE && cw == 1) STAMP(7);
      if (i < 9) Wd[i] = ld_nt(rd, i < 6 ? dof : dof2, (i / 3) * 8 * D * 2 + (i % 3) * 128);
      else Wo[i - 9] = ld_nt(ro, oof, ((i - 9) / 3) * 8 * D * 2 + (i % 3) * 128);
      SCHEDB();
    }
    lds_wait_ge(L + L_FLAG + F_H, 1);
#pragma unroll
    for (int j = 0; j < 3; ++j) Ah[j] = L[L_HV + 8 * j + (lane >> 3)] & mk;
    f32x4 acc[3];
#pragma unroll
    for (int t = 0; t < 3; ++t) {
      acc[t] = mfma(Ah[0], Wd[t], f32x4{0.f, 0.f, 0.f, 0.f});
      acc[t] = mfma(Ah[1], Wd[3 + t], acc[t]);
      acc[t] = mfma(Ah[2], Wd[6 + t], acc[t]);
    }
    if (cw == 1) STAMP(17);
    lds_wait_ge(L + L_FLAG + F_O, 1);
    const v4u A0 = L[L_OV + (lane >> 3)] & mk, A1 = L[L_OV + 8 + (lane >> 3)] & mk;
    if (cw == 1) STAMP(18);
#pragma unroll
    for (int t = 0; t < 3; ++t) acc[t] = mfma(A0, Wo[t], acc[t]);
#pragma unroll
    for (int t = 0; t < 3; ++t) acc[t] = mfma(A1, Wo[3 + t], acc[t]);
    const int po = WS_PART + ((4 * ct0 + 2 * ((lane >> 4) & 1) + (lane >> 5)) * NWG + b) * 128 + (lane & 7) * 16;
    const f32x2 f0 = fold<0>(acc[0]), f1 = fold<1>(acc[1]), f2 = fold<0>(acc[2]);
    st_pair(rs, po + par * (4 * NWG * 128), f2u(par ? f1[0] : f0[0]), f2u(par ? f1[1] : f0[1]), T0, T1);
    if (!par) st_pair(rs, po + 2 * (4 * NWG * 128), f2u(f2[0]), f2u(f2[1]), T0, T1);
    lds_signal(L + L_FLAG + F_PUB, lane);
    if (cw == 1) STAMP(11);
  }
  asm volatile(".Lfk_e2:");
  FLUSH();
}

extern "C" void kernel_launch(void* const* d_in, const int* in_sizes, int n_in, void* d_out, int out_size, void* d_ws, size_t ws_size,
                              hipStream_t stream) {
  fused_kernel<<<NWG, NTHR, 0, stream>>>((const u16*)d_in[0], (const u16*)d_in[1], (const u16*)d_in[2], (const u16*)d_in[3],
                                         (const u16*)d_in[4], (const u16*)d_in[6], (const u16*)d_in[7], (const u16*)d_in[8],
                                         (u16*)d_out, (unsigned char*)d_ws);
}
```

```cpp
#include <hip/hip_runtime.h>
#include <stdint.h>

#define CPRIO 3
#define APRIO 2
#define DLT 62
#define KV0 30
#define QK0 20
#define PVLAG 3
#define PVMAX 4
#define WIN 4
#define G0 2
#define TPRE 4
#define VGAP 24
#define TH 900
#define TQ 450
#define RDEL 60
#define PDEL 40
#define DBG 0

#define GAS __attribute__((address_space(1)))
#define LAS __attribute__((address_space(3)))
typedef unsigned u32;
typedef unsigned long long u64;
typedef unsigned short u16;
typedef u32 v4u __attribute__((ext_vector_type(4)));
typedef u32 v3u __attribute__((ext_vector_type(3)));
typedef u32 v2u __attribute__((ext_vector_type(2)));
typedef float f32x4 __attribute__((ext_vector_type(4)));
typedef float f32x2 __attribute__((ext_vector_type(2)));
typedef __bf16 bf16x8 __attribute__((ext_vector_type(8)));
typedef __bf16 bf16x2 __attribute__((ext_vector_type(2)));
typedef __amdgpu_buffer_rsrc_t rsrc_t;

constexpr int D = 576, SK = 8192, DH = 128, FF = 4608, QD = 4096;
constexpr int NWG = 256, NTHR = 768;
constexpr int NRED = 36, RED0 = NWG - NRED;
constexpr int GKS = 3, NGU = 72 * GKS;
constexpr int WS_EPOCH = 0;
constexpr int WS_Q = 4096;
constexpr int WS_REC = WS_Q + 16 * 4 * 128 * 16;
constexpr int WS_H = WS_REC + NWG * 16 * 8 * 16;
constexpr int WS_PART = WS_H + GKS * FF * 16;
constexpr int WS_END = WS_PART + NRED * NWG * 128;
constexpr int WS_DBG = 4 << 20;
constexpr u32 SPIN_CAP = 1u << 15;
constexpr int OOB = 0x40000000;
constexpr int L_FLAG = 0;
constexpr int L_TAG = 16;
constexpr int L_Q = 32;
constexpr int L_MX = L_Q + 128;
constexpr int L_LS = L_MX + 16;
constexpr int L_ORED = L_LS + 16;
constexpr int L_XT = L_ORED + 8 * 256;
constexpr int L_QP = L_XT + 640;
constexpr int L_GU = L_QP + 512;
constexpr int L_FIN = L_GU + 512;
constexpr int L_HV = L_FIN + 128;
constexpr int L_OV = L_HV + 32;
constexpr int L_WORDS = L_OV + 16;
enum { F_QRDY = 0, F_MAXC = 2, F_DONE = 3, F_GU = 4, F_PUB = 5, F_FIN = 6, F_XN = 7, F_QP = 8, F_LS = 9, F_H = 10, F_O = 11 };

__device__ __forceinline__ float u2f(u32 x) { return __builtin_bit_cast(float, x); }
__device__ __forceinline__ u32 f2u(float x) { return __builtin_bit_cast(u32, x); }
__device__ __forceinline__ float bf_lo(u32 x) { return u2f(x << 16); }
__device__ __forceinline__ float bf_hi(u32 x) { return u2f(x & 0xffff0000u); }
__device__ __forceinline__ u32 cvtpk(float lo, float hi) {
  f32x2 v = {lo, hi};
  return __builtin_bit_cast(u32, __builtin_convertvector(v, bf16x2));
}
__device__ __forceinline__ float rbf(float x) { return (float)(__bf16)x; }
__device__ __forceinline__ float rcp(float x) { return __builtin_amdgcn_rcpf(x); }
__device__ __forceinline__ float opq(float x) { asm volatile("" : "+v"(x)); return x; }
template <int CTRL> __device__ __forceinline__ u32 dppu(u32 x) { return (u32)__builtin_amdgcn_mov_dpp((int)x, CTRL, 0xf, 0xf, true); }
template <int CTRL> __device__ __forceinline__ float dpp(float x) { return u2f(dppu<CTRL>(f2u(x))); }
constexpr int XOR1 = 0xB1, XOR2 = 0x4E, HMIR = 0x141, XOR8 = 0x128;
__device__ __forceinline__ float xrow_max(float x) {
  auto s = __builtin_amdgcn_permlane16_swap(f2u(x), f2u(x), false, false);
  x = fmaxf(u2f(s[0]), u2f(s[1]));
  auto t = __builtin_amdgcn_permlane32_swap(f2u(x), f2u(x), false, false);
  return fmaxf(u2f(t[0]), u2f(t[1]));
}
__device__ __forceinline__ float xrow_sum(float x) {
  auto s = __builtin_amdgcn_permlane16_swap(f2u(x), f2u(x), false, false);
  x = u2f(s[0]) + u2f(s[1]);
  auto t = __builtin_amdgcn_permlane32_swap(f2u(x), f2u(x), false, false);
  return u2f(t[0]) + u2f(t[1]);
}
__device__ __forceinline__ float row_sum(float x) {
  x = opq(x);
  x += dpp<XOR1>(x); x += dpp<XOR2>(x); x += dpp<HMIR>(x); x += dpp<XOR8>(x); return x;
}
__device__ __forceinline__ float row_max(float x) {
  x = fmaxf(x, dpp<XOR1>(x)); x = fmaxf(x, dpp<XOR2>(x)); x = fmaxf(x, dpp<HMIR>(x)); x = fmaxf(x, dpp<XOR8>(x)); return x;
}
__device__ __forceinline__ f32x4 mfma(v4u a, v4u b, f32x4 c) {
  return __builtin_amdgcn_mfma_f32_16x16x32_bf16(__builtin_bit_cast(bf16x8, a), __builtin_bit_cast(bf16x8, b), c, 0, 0, 0);
}
template <int ODD> __device__ __forceinline__ f32x2 fold(f32x4 a) {
  auto s0 = __builtin_amdgcn_permlane32_swap(f2u(a[0]), f2u(a[2]), false, false);
  auto s1 = __builtin_amdgcn_permlane32_swap(f2u(a[1]), f2u(a[3]), false, false);
  f32x2 r;
  if (ODD) { r[0] = dpp<XOR8>(u2f(s0[0])) + u2f(s0[1]); r[1] = dpp<XOR8>(u2f(s1[0])) + u2f(s1[1]); }
  else { r[0] = u2f(s0[0]) + dpp<XOR8>(u2f(s0[1])); r[1] = u2f(s1[0]) + dpp<XOR8>(u2f(s1[1])); }
  return r;
}
#define LGKM0() asm volatile("s_waitcnt lgkmcnt(0)" ::: "memory")
#define SCHEDB() __builtin_amdgcn_sched_barrier(0)
template <int N> __device__ __forceinline__ void vmwait() { asm volatile("s_waitcnt vmcnt(%0)" ::"i"(N) : "memory"); }
__device__ __forceinline__ u32 lds_ld(LAS u32* p) { return __hip_atomic_load(p, __ATOMIC_RELAXED, __HIP_MEMORY_SCOPE_WORKGROUP); }
__device__ __forceinline__ void lds_wait_ge(LAS u32* p, u32 n) {
#pragma unroll 1
  for (u32 i = 0; i < (1u << 20); ++i) {
    if ((u32)__builtin_amdgcn_readfirstlane(lds_ld(p)) >= n) break;
    __builtin_amdgcn_s_sleep(1);
  }
  asm volatile("" ::: "memory");
}
__device__ __forceinline__ u32 lds_arrive(LAS u32* p, int lane) {
  LGKM0();
  u32 r = 0;
  if (lane == 0) r = __hip_atomic_fetch_add(p, 1u, __ATOMIC_RELAXED, __HIP_MEMORY_SCOPE_WORKGROUP);
  r = __builtin_amdgcn_readfirstlane(r);
  asm volatile("" ::: "memory");
  return r;
}
__device__ __forceinline__ void lds_signal(LAS u32* p, int lane) {
  LGKM0();
  if (lane == 0) __hip_atomic_fetch_add(p, 1u, __ATOMIC_RELAXED, __HIP_MEMORY_SCOPE_WORKGROUP);
  asm volatile("" ::: "memory");
}
__device__ __forceinline__ u32 now() { return (u32)__builtin_amdgcn_s_memrealtime(); }
template <int SL> __device__ __forceinline__ void wait_sl(u32 t) {
  asm volatile("s_branch .Lwt%=\n"
               ".Lws%=:\n\ts_sleep %1\n"
               ".Lwt%=:\n\ts_memrealtime vcc\n\ts_waitcnt lgkmcnt(0)\n\ts_sub_i32 vcc_hi, %0, vcc_lo\n\ts_cmp_gt_i32 vcc_hi, 0\n\ts_cbranch_scc1 .Lws%="
               : : "s"(t), "i"(SL) : "memory", "scc", "vcc");
}
__device__ __forceinline__ void wait_until(u32 t) { wait_sl<1>(t); }
__device__ __forceinline__ void wait_long(u32 t) { wait_sl<12>(t - 45); wait_sl<1>(t); }
template <int I> struct IC { static constexpr int value = I; };
template <int I, int N, class F> __device__ __forceinline__ void static_for(F&& f) {
  if constexpr (I < N) { f(IC<I>{}); static_for<I + 1, N>(f); }
}
constexpr int pv_done(int i) {
  if (i < QK0 + 6) return 0;
  if (i >= 32) return 16;
  const int p = pv_done(i - 1), a = i - 16 - PVLAG + 1;
  return a < p ? p : (a < p + PVMAX ? a : p + PVMAX);
}
__device__ __forceinline__ float dot2(u32 a, u32 b, float c) {
  return __builtin_amdgcn_fdot2_f32_bf16(__builtin_bit_cast(bf16x2, a), __builtin_bit_cast(bf16x2, b), c, false);
}
constexpr int SL16(int n16) { return (n16 * DLT) >> 8; }
constexpr int GSL = 3 * 16 * 27 / 32;
constexpr int tail_pos(int i) { return 3 * (i < 6 ? 16 * i : i < 9 ? 96 + 4 * (i - 6) : 108 + 16 * (i - 9)); }
constexpr int kv_pos(int i) {
  const int g = i - G0 < 0 ? 0 : (i - G0 > 16 ? 16 : i - G0);
  return 128 * i + GSL * g + (i >= VGAP ? tail_pos(TPRE) : 0);
}
constexpr int AUX_SC1 = 16, AUX_NT = 2;
__device__ __forceinline__ rsrc_t mkrs(const void* p, int bytes) { return __builtin_amdgcn_make_buffer_rsrc((void*)p, 0, bytes, 0x00020000); }
__device__ __forceinline__ v4u ld_nt(rsrc_t r, int voff, int soff) { return __builtin_amdgcn_raw_buffer_load_b128(r, voff, soff, AUX_NT); }
__device__ __forceinline__ v4u ld_sc1(rsrc_t rs, int off) { return __builtin_amdgcn_raw_buffer_load_b128(rs, off, 0, AUX_SC1); }
__device__ __forceinline__ void st_pair(rsrc_t rs, int off, u32 a, u32 b, u32 T0, u32 T1) {
  v4u v = {a, T0, b, T1};
  __builtin_amdgcn_raw_buffer_store_b128(v, rs, off, 0, AUX_SC1);
}
#define STAMP(k) do { } while (0)
#define FLUSH() do { } while (0)

__global__ void __launch_bounds__(64) tiny_pre(unsigned char* ws_) {
  unsigned long long tA_;
  asm volatile("s_memrealtime %0\n\ts_waitcnt lgkmcnt(0)" : "=s"(tA_));
  if (threadIdx.x == 0) *(volatile u32*)(ws_ + (8 << 20)) = (u32)tA_;
}
__global__ void __launch_bounds__(64) tiny_post(unsigned char* ws_, int off) {
  u32 mn = 0x7fffffff;
  const u32 n0 = now();
  u32 v = 0;
  if (off == 0) v = *(volatile u32*)(ws_ + (8 << 20));
  else {
    int best = 0;
    for (int k = 0; k < 4; ++k) { u32 t = *(volatile u32*)(ws_ + (9 << 20) + (threadIdx.x * 4 + k) * 4); int d = (int)(n0 - t); best = d > best ? d : best; }
    for (int o = 32; o; o >>= 1) { int x = __shfl_xor(best, o); best = x > best ? x : best; }
    v = n0 - best;
  }
  v = __builtin_amdgcn_readfirstlane(v) + 6000;
  if ((int)(v - n0) > 0 && (int)(v - n0) < 7000) wait_long(v);
}
__global__ void __launch_bounds__(NTHR, 3) __attribute__((aligned(4096)))
fused_kernel(const u16* __restrict__ x_, const u16* __restrict__ kc_, const u16* __restrict__ vc_, const u16* __restrict__ wq_,
             const u16* __restrict__ wo_, const u16* __restrict__ wg_, const u16* __restrict__ wu_, const u16* __restrict__ wd_,
             u16* __restrict__ out_, unsigned char* ws_) {
  __shared__ __attribute__((aligned(16))) u32 lds_raw[L_WORDS];
  LAS u32* L = (LAS u32*)lds_raw;
  unsigned long long tA_;
  asm volatile("s_memrealtime %0" : "=s"(tA_));
  u32 cpf;
  {
    u64 pc; u32 len;
    asm volatile("s_getpc_b64 %0\n.Lfk_pc:\n\ts_mov_b32 %1, .Lfk_e0-.Lfk_pc\n\ts_max_i32 %1, %1, .Lfk_e1-.Lfk_pc\n\ts_max_i32 %1, %1, .Lfk_e2-.Lfk_pc"
                 : "=s"(pc), "=s"(len) : : "scc");
    cpf = __builtin_amdgcn_raw_buffer_load_b32(__builtin_amdgcn_make_buffer_rsrc((void*)pc, 0, len, 0x00020000), threadIdx.x * 64, 0, 0);
    SCHEDB();
  }
  const int b = blockIdx.x;
  const int lane = threadIdx.x & 63;
  const int wave = __builtin_amdgcn_readfirstlane(threadIdx.x >> 6);
  const int h = (b & 7) * 2 + ((b >> 3) & 1), s = b >> 4;
  const int cw = wave - 8;
  asm volatile("" ::"s"(x_), "s"(kc_), "s"(vc_), "s"(wq_), "s"(wo_), "s"(wg_), "s"(wu_), "s"(wd_), "s"(out_), "s"(ws_));
  const rsrc_t rs = mkrs(ws_, WS_END);
  v4u W0[6], xa, xb;
  v2u ep;
  const size_t kvbase = ((size_t)(h * SK + s * 512) * DH) * 2;
  const rsrc_t rk = mkrs((const char*)kc_ + kvbase, 512 * DH * 2), rv = mkrs((const char*)vc_ + kvbase, 512 * DH * 2);
  const int ko = wave * 1024 + (((lane >> 2) & 3) * 16 + (lane >> 4) * 4 + (lane & 3)) * 16;
  const int r0 = 144 * (s & 3) + 48 * (cw - 1);
  {
    const rsrc_t rq = mkrs(wave < 8 ? (const char*)kc_ + kvbase : (const char*)wq_, wave < 8 ? 512 * DH * 2 : wave == 8 ? 0 : D * QD * 2);
    const int qo = (r0 + (lane >> 3)) * (QD * 2) + (256 * h + 64 * (s >> 2)) * 2 + (lane & 7) * 16;
    W0[0] = ld_nt(rq, wave < 8 ? ko : qo, 0);
    if (wave >= 8) {
      const rsrc_t rx = mkrs(x_, wave == 8 ? D * 2 : 0), re = mkrs(ws_, wave == 8 ? 8 : 0);
      xa = __builtin_amdgcn_raw_buffer_load_b128(rx, lane * 16, 0, 0);
      xb = __builtin_amdgcn_raw_buffer_load_b128(rx, 1024 + (lane & 7) * 16, 0, 0);
      ep = __builtin_amdgcn_raw_buffer_load_b64(re, WS_EPOCH, 0, AUX_SC1);
#pragma unroll
      for (int i = 1; i < 6; ++i) W0[i] = ld_nt(rq, qo, i * 8 * QD * 2);
    }
  }
  SCHEDB();
  const u32 t0 = now();
  if (wave == 8) {
    if (lane < 16) L[L_FLAG + lane] = 0u;
    LGKM0();
  }
  v4u mk;
  {
    const int e = lane & 7;
    const u32 hv = (e & 1) ? 0xffff0000u : 0x0000ffffu;
    mk[0] = (e >> 1) == 0 ? hv : 0u; mk[1] = (e >> 1) == 1 ? hv : 0u; mk[2] = (e >> 1) == 2 ? hv : 0u; mk[3] = (e >> 1) == 3 ? hv : 0u;
  }
  const int par = (lane >> 3) & 1;
  LAS u32* xt = L + L_XT;
  __builtin_amdgcn_s_barrier();
  u32 T0 = 0, T1 = 0;

  if (wave == 8) {
    __builtin_amdgcn_s_setprio(CPRIO);
    float v[16];
#pragma unroll
    for (int k = 0; k < 4; ++k) { v[2 * k] = bf_lo(xa[k]); v[2 * k + 1] = bf_hi(xa[k]); v[8 + 2 * k] = bf_lo(xb[k]); v[9 + 2 * k] = bf_hi(xb[k]); }
    const bool tail = lane < 8;
    float s0 = ((v[0] + v[1]) + (v[2] + v[3])) + ((v[4] + v[5]) + (v[6] + v[7]));
    float s1 = ((v[8] + v[9]) + (v[10] + v[11])) + ((v[12] + v[13]) + (v[14] + v[15]));
    const float mean = xrow_sum(row_sum(s0 + (tail ? s1 : 0.f))) * (1.f / D);
    float q0 = 0.f, q1 = 0.f;
#pragma unroll
    for (int k = 0; k < 8; ++k) { float d0 = v[k] - mean, d1 = v[8 + k] - mean; q0 += d0 * d0; q1 += d1 * d1; }
    const float var = xrow_sum(row_sum(q0 + (tail ? q1 : 0.f))) * (1.f / D);
    const float mb = rbf(mean), vb = rbf(var);
    const float rden = rcp(rbf(__builtin_amdgcn_sqrtf(rbf(vb + 1e-5f))));
#pragma unroll
    for (int k = 0; k < 8; ++k) {
      const float a = rbf(v[k] - mb) * rden;
      xt[k * 80 + lane] = cvtpk(a, a);
    }
    if (tail) {
#pragma unroll
      for (int k = 0; k < 8; ++k) {
        const float a = rbf(v[8 + k] - mb) * rden;
        xt[k * 80 + 64 + lane] = cvtpk(a, a);
      }
    }
    const u64 e1 = (((u64)(u32)__builtin_amdgcn_readfirstlane(ep[1]) << 32) | (u32)__builtin_amdgcn_readfirstlane(ep[0])) + 1;
    T0 = (u32)e1;
    T1 = (u32)(e1 >> 32) + T0 * 0x9E3779B1u;
    if (lane == 0) *(LAS v2u*)(L + L_TAG) = v2u{T0, T1};
    lds_signal(L + L_FLAG + F_XN, lane);
    STAMP(14);
  } else if (wave > 8) {
    __builtin_amdgcn_s_setprio(CPRIO);
    lds_wait_ge(L + L_FLAG + F_XN, 1);
    const LAS u32* xr = xt + (lane >> 3) * 80 + (r0 >> 3);
    const v2u tg = *(const LAS v2u*)(L + L_TAG);
    f32x4 a0 = {0.f, 0.f, 0.f, 0.f}, a1 = {0.f, 0.f, 0.f, 0.f};
#pragma unroll
    for (int i = 0; i < 6; i += 2) {
      const v2u xv = *(const LAS v2u*)(xr + i);
      a0 = mfma(xv[0] & mk, W0[i], a0);
      a1 = mfma(xv[1] & mk, W0[i + 1], a1);
    }
    a0 += a1;
    T0 = __builtin_amdgcn_readfirstlane(tg[0]);
    T1 = __builtin_amdgcn_readfirstlane(tg[1]);
    if ((lane >> 5) == par) {
      LAS u32* g = L + L_QP + (8 * (lane & 7) + 4 * ((lane >> 4) & 1)) * 8 + (cw - 1) * 2 + par;
#pragma unroll
      for (int r = 0; r < 4; ++r) g[r * 8] = f2u(a0[r]);
    }
    const u32 old = lds_arrive(L + L_FLAG + F_QP, lane);
    if (old == 2) {
      const LAS u32* qp = L + L_QP + (lane & 31) * 16;
      const f32x4 a = *(const LAS f32x4*)qp, a2 = *(const LAS f32x4*)(qp + 8);
      const f32x2 c = *(const LAS f32x2*)(qp + 4), c2 = *(const LAS f32x2*)(qp + 12);
      const float q = ((a[0] + a[1]) + (a[2] + a[3])) + (c[0] + c[1]);
      const float qn = ((a2[0] + a2[1]) + (a2[2] + a2[3])) + (c2[0] + c2[1]);
      if (lane < 32) st_pair(rs, WS_Q + ((h * 4 + (s & 3)) * 128 + 32 * (s >> 2) + lane) * 16, f2u(q), f2u(qn), T0, T1);
      STAMP(5);
    }
  }

  if (wave < 8) {
    if (wave == 0) STAMP(0);
    v4u K[16], V[16];
    K[0] = W0[0];
    const int vo = wave * 1024 + lane * 16;
    const u32 tb = t0 + KV0 + ((wave * DLT) >> 4);
    const int hb = par;
    const bool t1 = (lane >> 2) & 1;
    v4u Be, Bo;
    float sc[8];
    u32 pk[8], tw[2];
    float m = -INFINITY;
    f32x4 acc0 = {0.f, 0.f, 0.f, 0.f}, acc1 = {0.f, 0.f, 0.f, 0.f};
    __builtin_amdgcn_s_setprio(APRIO);
    static_for<1, 33>([&](auto ic) __attribute__((always_inline)) {
      constexpr int i = decltype(ic)::value;
      if constexpr (i < 32) {
        wait_until(tb + SL16(kv_pos(i)));
        if constexpr (i > WIN) vmwait<WIN>();
        if constexpr (i == 1) V[0] = ld_nt(rv, vo, 0);
        else if constexpr (i < 17) K[i - 1] = ld_nt(rk, ko, (i - 1) * 8192);
        else V[i - 16] = ld_nt(rv, vo, (i - 16) * 8192);
        SCHEDB();
      }
      if constexpr (i == WIN + 1) asm volatile("" ::"v"(cpf));
      if (i == 31 && wave == 7) STAMP(1);
      if constexpr (i == QK0) {
        lds_wait_ge(L + L_FLAG + F_QRDY, 1);
        if (wave == 0) STAMP(2);
        const v4u qf = *(const LAS v4u*)(L + L_Q + hb * 64 + (lane >> 4) * 16 + (lane & 3) * 4);
        const v4u zero4 = {0u, 0u, 0u, 0u};
        Be = t1 ? zero4 : qf; Bo = t1 ? qf : zero4;
      }
      if constexpr (i >= QK0 && i < QK0 + 4) {
#pragma unroll
        for (int j = 2 * (i - QK0); j < 2 * (i - QK0) + 2; ++j) {
          f32x4 d = {0.f, 0.f, 0.f, 0.f};
          d = mfma(K[2 * j], Be, d);
          d = mfma(K[2 * j + 1], Bo, d);
          float v = (dpp<0x00>(d[0]) + dpp<0x55>(d[1])) + (dpp<0xAA>(d[2]) + dpp<0xFF>(d[3]));
          v = rbf(rbf(v) * 0.08838834764831845f);
          sc[j] = v;
          m = fmaxf(m, v);
        }
      }
      if constexpr (i == QK0 + 3) {
        m = fmaxf(m, dpp<HMIR>(m));
        m = xrow_max(m);
        if ((lane & 55) == 0) L[L_MX + hb * 8 + wave] = f2u(m);
        lds_signal(L + L_FLAG + F_MAXC, lane);
      }
      if constexpr (i == QK0 + 5) {
        lds_wait_ge(L + L_FLAG + F_MAXC, 8);
        const f32x4 a = *(const LAS f32x4*)(L + L_MX + hb * 8), c = *(const LAS f32x4*)(L + L_MX + hb * 8 + 4);
        const float M = fmaxf(fmaxf(fmaxf(a[0], a[1]), fmaxf(a[2], a[3])), fmaxf(fmaxf(c[0], c[1]), fmaxf(c[2], c[3])));
        float ls = 0.f;
#pragma unroll
        for (int j = 0; j < 8; ++j) {
          const float p = __expf(sc[j] - M);
          ls += p;
          pk[j] = cvtpk(p, p);
        }
        ls += dpp<HMIR>(ls);
        ls = xrow_sum(ls);
        if ((lane & 55) == 0) L[L_LS + hb * 8 + wave] = f2u(ls);
        lds_signal(L + L_FLAG + F_LS, lane);
        if (wave == 0) STAMP(3);
      }
      if constexpr (i == QK0 + 8) {
        lds_wait_ge(L + L_FLAG + F_LS, 8);
        const v2u tg = *(const LAS v2u*)(L + L_TAG);
        T0 = __builtin_amdgcn_readfirstlane(tg[0]); T1 = __builtin_amdgcn_readfirstlane(tg[1]);
#pragma unroll
        for (int hh = 0; hh < 2; ++hh) {
          const LAS u32* sp = L + ((lane & 4) ? L_LS : L_MX) + hh * 8;
          const f32x4 a = *(const LAS f32x4*)sp, c = *(const LAS f32x4*)(sp + 4);
          const float Mh = fmaxf(fmaxf(fmaxf(a[0], a[1]), fmaxf(a[2], a[3])), fmaxf(fmaxf(c[0], c[1]), fmaxf(c[2], c[3])));
          const float Lh = ((a[0] + a[1]) + (a[2] + a[3])) + ((c[0] + c[1]) + (c[2] + c[3]));
          tw[hh] = (T1 & 0xffffff00u) | ((f2u((lane & 4) ? Lh : Mh) >> ((lane & 3) * 8)) & 0xffu);
        }
      }
      if constexpr (i >= QK0 + 6) {
        constexpr int lo = pv_done(i - 1), hi = pv_done(i);
#pragma unroll
        for (int j = lo; j < hi; ++j) {
          const u32 pp = pk[j >> 1], pm = dppu<HMIR>(pp);
          if (j & 1) acc1 = mfma(v4u{pm & mk[0], pm & mk[1], pp & mk[2], pp & mk[3]}, V[j], acc1);
          else acc0 = mfma(v4u{pp & mk[0], pp & mk[1], pm & mk[2], pm & mk[3]}, V[j], acc0);
        }
      }
      SCHEDB();
    });
    acc0 += acc1;
    *(LAS f32x4*)(L + L_ORED + wave * 256 + (lane >> 5) * 128 + 8 * (lane & 15) + 4 * ((lane >> 4) & 1)) = acc0;
    if (wave == 7) STAMP(15);
    const u32 old = lds_arrive(L + L_FLAG + F_DONE, lane);
    if (old == 7) {
      f32x2 r[2][8];
#pragma unroll
      for (int k = 0; k < 8; ++k) {
        r[0][k] = *(const LAS f32x2*)(L + L_ORED + k * 256 + 2 * lane);
        r[1][k] = *(const LAS f32x2*)(L + L_ORED + k * 256 + 128 + 2 * lane);
      }
#pragma unroll
      for (int hh = 0; hh < 2; ++hh) {
        const f32x2 o = ((r[hh][0] + r[hh][1]) + (r[hh][2] + r[hh][3])) + ((r[hh][4] + r[hh][5]) + (r[hh][6] + r[hh][7]));
        const int rec = (h * 16 + hh * 8 + (lane >> 3)) * 16 + s;
        st_pair(rs, WS_REC + (rec * 8 + (lane & 7)) * 16, f2u(o[0]), f2u(o[1]), T0, tw[hh]);
      }
      STAMP(4);
    }
    if (b >= RED0) {
      const int r = b - RED0;
      const int col = 64 * (r >> 2) + 4 * ((r >> 1) & 1) + 2 * (r & 1) + 8 * ((lane >> 1) & 7) + (lane & 1);
      const u32 xo = x_[col];
      const int pb = WS_PART + (r * NWG + wave * 32) * 128 + lane * 16;
      v4u g[4];
      bool ok[4];
#pragma unroll
      for (int k = 0; k < 4; ++k) { g[k] = v4u{0u, 0u, 0u, 0u}; ok[k] = false; }
      lds_wait_ge(L + L_FLAG + F_PUB, 3);
      wait_until(now() + PDEL);
      if (wave == 0) STAMP(12);
#pragma unroll 1
      for (u32 it = 0; it < SPIN_CAP; ++it) {
#pragma unroll
        for (int k = 0; k < 4; ++k)
          if (!ok[k]) g[k] = ld_sc1(rs, pb + k * 1024);
        bool all = true;
#pragma unroll
        for (int k = 0; k < 4; ++k) { ok[k] = ok[k] || (g[k][1] == T0 && g[k][3] == T1); all = all && ok[k]; }
        if (__all(all)) break;
        asm volatile("" ::: "memory");
      }
      float a0 = (u2f(g[0][0]) + u2f(g[1][0])) + (u2f(g[2][0]) + u2f(g[3][0]));
      float a1 = (u2f(g[0][2]) + u2f(g[1][2])) + (u2f(g[2][2]) + u2f(g[3][2]));
      a0 += dpp<XOR8>(a0); a1 += dpp<XOR8>(a1);
      a0 = xrow_sum(a0); a1 = xrow_sum(a1);
      if (lane < 8) *(LAS v2u*)(L + L_FIN + wave * 16 + 2 * lane) = v2u{f2u(a0), f2u(a1)};
      const u32 old2 = lds_arrive(L + L_FLAG + F_FIN, lane);
      if (old2 == 7) {
        if (lane < 16) {
          const LAS float* f = (const LAS float*)(L + L_FIN) + lane;
          const float A = ((f[0] + f[16]) + (f[32] + f[48])) + ((f[64] + f[80]) + (f[96] + f[112]));
          const float o = bf_lo(xo) + A;
          out_[col] = (u16)cvtpk(o, o);
        }
        if (r == 0 && lane == 0) {
          const v2u e1 = {T0, T1 - T0 * 0x9E3779B1u};
          __builtin_amdgcn_raw_buffer_store_b64(e1, rs, WS_EPOCH, 0, AUX_SC1);
        }
        asm volatile(".Lfk_e0:");
        STAMP(13);
      }
    }
    FLUSH();
    return;
  }

  asm volatile("" ::"v"(cpf));
  if (wave == 8) {
    if (lane == 0) *(volatile u32*)(ws_ + (9 << 20) + b * 4) = (u32)tA_;
    u32 tch;
    {
      const char* p = (lane & 1) ? (const char*)wd_ + (18 * b) * (D * 2) : (const char*)wo_ + (256 * h + 16 * s) * (D * 2);
      if (lane & 2) p = (const char*)out_;
      tch = *(const GAS u32*)p;
    }
    wait_long(t0 + TQ);
    asm volatile("" ::"v"(tch));
    v4u g[8];
    bool ok = false;
#pragma unroll 1
    for (u32 it = 0; it < SPIN_CAP; ++it) {
      if (!ok) {
#pragma unroll
        for (int k = 0; k < 8; ++k) g[k] = ld_sc1(rs, WS_Q + (512 * h + 64 * k + lane) * 16);
      }
      ok = true;
#pragma unroll
      for (int k = 0; k < 8; ++k) ok = ok && g[k][1] == T0 && g[k][3] == T1;
      if (__all(ok)) break;
      __builtin_amdgcn_s_sleep(16);
      asm volatile("" ::: "memory");
    }
#pragma unroll
    for (int k = 0; k < 2; ++k) {
      const float qa = (u2f(g[k][0]) + u2f(g[2 + k][0])) + (u2f(g[4 + k][0]) + u2f(g[6 + k][0]));
      const float qb = (u2f(g[k][2]) + u2f(g[2 + k][2])) + (u2f(g[4 + k][2]) + u2f(g[6 + k][2]));
      L[L_Q + 64 * k + lane] = cvtpk(qa, qb);
    }
    lds_signal(L + L_FLAG + F_QRDY, lane);
    STAMP(6);
    {
      wait_long(t0 + TH);
      const bool act = lane < 18;
      const int ho = WS_H + (18 * b + lane) * 16;
      v4u gh[GKS];
      ok = !act;
#pragma unroll
      for (int k = 0; k < GKS; ++k) gh[k] = v4u{0u, 0u, 0u, 0u};
#pragma unroll 1
      for (u32 it = 0; it < SPIN_CAP; ++it) {
        if (!ok) {
#pragma unroll
          for (int k = 0; k < GKS; ++k) gh[k] = ld_sc1(rs, ho + k * FF * 16);
          ok = true;
#pragma unroll
          for (int k = 0; k < GKS; ++k) ok = ok && gh[k][1] == T0 && gh[k][3] == T1;
        }
        if (__all(ok)) break;
        __builtin_amdgcn_s_sleep(16);
        asm volatile("" ::: "memory");
      }
      float gs = u2f(gh[0][0]), us = u2f(gh[0][2]);
#pragma unroll
      for (int k = 1; k < GKS; ++k) { gs += u2f(gh[k][0]); us += u2f(gh[k][2]); }
      const float gb = rbf(gs), ub = rbf(us);
      float hv = rbf(gb * rcp(1.f + __expf(-gb))) * ub;
      if (!act) hv = 0.f;
      if (lane < 24) L[L_HV + lane] = cvtpk(hv, hv);
      lds_signal(L + L_FLAG + F_H, lane);
      STAMP(9);
    }
    {
      wait_long(t0 + KV0 + SL16(kv_pos(32)));
      lds_wait_ge(L + L_FLAG + F_DONE, 8);
      wait_until(now() + RDEL);
      const int off = WS_REC + (h * 16 + s) * 2048 + lane * 16;
      v4u r0 = {0u, 0u, 0u, 0u}, r1 = r0;
      bool ok0 = false, ok1 = false;
      const u32 T1h = T1 >> 8;
      STAMP(19);
#pragma unroll 1
      for (u32 it = 0; it < SPIN_CAP; ++it) {
        if (!ok0) r0 = ld_sc1(rs, off);
        if (!ok1) r1 = ld_sc1(rs, off + 1024);
        ok0 = r0[1] == T0 && (r0[3] >> 8) == T1h;
        ok1 = r1[1] == T0 && (r1[3] >> 8) == T1h;
        if (__all(ok0 && ok1)) break;
        __builtin_amdgcn_s_sleep(4);
        asm volatile("" ::: "memory");
      }
      STAMP(10);
      const int sh = (lane & 3) * 8;
      u32 s0 = (r0[3] & 0xffu) << sh, s1 = (r1[3] & 0xffu) << sh;
      s0 |= dppu<XOR1>(s0); s1 |= dppu<XOR1>(s1);
      s0 |= dppu<XOR2>(s0); s1 |= dppu<XOR2>(s1);
      const u32 x0 = dppu<HMIR>(s0), x1 = dppu<HMIR>(s1);
      const bool up = lane & 4;
      const float m0 = u2f(up ? x0 : s0), l0 = u2f(up ? s0 : x0), m1 = u2f(up ? x1 : s1), l1 = u2f(up ? s1 : x1);
      float M = fmaxf(m0, m1);
      M = fmaxf(M, dpp<XOR8>(M));
      M = xrow_max(M);
      const float w0 = __expf(m0 - M), w1 = __expf(m1 - M);
      float ls = opq(w0 * l0) + opq(w1 * l1), oa = opq(w0 * u2f(r0[0])) + opq(w1 * u2f(r1[0])), ob = opq(w0 * u2f(r0[2])) + opq(w1 * u2f(r1[2]));
      ls += dpp<XOR8>(ls); oa += dpp<XOR8>(oa); ob += dpp<XOR8>(ob);
      ls = xrow_sum(ls); oa = xrow_sum(oa); ob = xrow_sum(ob);
      const float rl = rcp(ls);
      oa *= rl; ob *= rl;
      if (lane < 8) *(LAS v2u*)(L + L_OV + 2 * lane) = v2u{cvtpk(oa, oa), cvtpk(ob, ob)};
      lds_signal(L + L_FLAG + F_O, lane);
      asm volatile(".Lfk_e1:");
    }
    FLUSH();
    return;
  }
  const int c = cw - 1;
  if (b < NGU) {
    const int gcg = b / GKS, gks = b % GKS;
    const int grp0 = 24 * gks + 8 * c;
    const rsrc_t rg = mkrs(wg_, D * FF * 2), ru = mkrs(wu_, D * FF * 2);
    const int go = (grp0 * 8 + (lane >> 3)) * (FF * 2) + gcg * 128 + (lane & 7) * 16;
    const u32 tg = t0 + KV0 + SL16(128 * G0) + c * SL16((128 + GSL) / 3);
    v4u Wg[16];
#pragma unroll
    for (int i = 0; i < 16; ++i) {
      wait_until(tg + SL16((128 + GSL) * i));
      Wg[i] = ld_nt((i & 1) ? ru : rg, go, (i >> 1) * 8 * FF * 2);
      SCHEDB();
    }
    const LAS u32* xr = xt + (lane >> 3) * 80 + grp0;
    f32x4 ag = {0.f, 0.f, 0.f, 0.f}, au = {0.f, 0.f, 0.f, 0.f};
#pragma unroll
    for (int i = 0; i < 8; ++i) {
      const v4u A = xr[i] & mk;
      ag = mfma(A, Wg[2 * i], ag);
      au = mfma(A, Wg[2 * i + 1], au);
    }
    const f32x2 fg = fold<0>(ag), fu = fold<1>(au);
    const int cc = 8 * (lane & 7) + 4 * ((lane >> 4) & 1) + 2 * (lane >> 5);
    LAS u32* g = L + L_GU + (par * 64 + cc) * 4 + c;
    g[0] = f2u(par ? fu[0] : fg[0]);
    g[4] = f2u(par ? fu[1] : fg[1]);
    const u32 old = lds_arrive(L + L_FLAG + F_GU, lane);
    if (old == 2) {
      const f32x4 a = *(const LAS f32x4*)(L + L_GU + lane * 4), c4 = *(const LAS f32x4*)(L + L_GU + 256 + lane * 4);
      st_pair(rs, WS_H + (gks * FF + gcg * 64 + lane) * 16, f2u((a[0] + a[1]) + a[2]), f2u((c4[0] + c4[1]) + c4[2]), T0, T1);
      STAMP(8);
    }
  }
  {
    const int ct0 = 3 * c;
    const rsrc_t rd = mkrs(wd_, FF * D * 2), ro = mkrs(wo_, QD * D * 2);
    const int dof = (18 * b + (lane >> 3)) * (D * 2) + ct0 * 128 + (lane & 7) * 16;
    const int dof2 = lane < 16 ? dof : OOB;
    const int oof = (256 * h + 16 * s + (lane >> 3)) * (D * 2) + ct0 * 128 + (lane & 7) * 16;
    v4u Wd[9], Wo[6], Ah[3];
    const u32 tt = t0 + KV0 + c * SL16(16);
#pragma unroll
    for (int i = 0; i < 15; ++i) {
      const u32 tw_ = tt + SL16((i < TPRE ? kv_pos(VGAP) : kv_pos(32)) - tail_pos(TPRE) + tail_pos(i));
      if (i == 0 || i == TPRE) wait_long(tw_); else wait_until(tw_);
      if (i == TPRE && cw == 1) STAMP(7);
      if (i < 9) Wd[i] = ld_nt(rd, i < 6 ? dof : dof2, (i / 3) * 8 * D * 2 + (i % 3) * 128);
      else Wo[i - 9] = ld_nt(ro, oof, ((i - 9) / 3) * 8 * D * 2 + (i % 3) * 128);
      SCHEDB();
    }
    lds_wait_ge(L + L_FLAG + F_H, 1);
#pragma unroll
    for (int j = 0; j < 3; ++j) Ah[j] = L[L_HV + 8 * j + (lane >> 3)] & mk;
    f32x4 acc[3];
#pragma unroll
    for (int t = 0; t < 3; ++t) {
      acc[t] = mfma(Ah[0], Wd[t], f32x4{0.f, 0.f, 0.f, 0.f});
      acc[t] = mfma(Ah[1], Wd[3 + t], acc[t]);
      acc[t] = mfma(Ah[2], Wd[6 + t], acc[t]);
    }
    if (cw == 1) STAMP(17);
    lds_wait_ge(L + L_FLAG + F_O, 1);
    const v4u A0 = L[L_OV + (lane >> 3)] & mk, A1 = L[L_OV + 8 + (lane >> 3)] & mk;
    if (cw == 1) STAMP(18);
#pragma unroll
    for (int t = 0; t < 3; ++t) acc[t] = mfma(A0, Wo[t], acc[t]);
#pragma unroll
    for (int t = 0; t < 3; ++t) acc[t] = mfma(A1, Wo[3 + t], acc[t]);
    const int po = WS_PART + ((4 * ct0 + 2 * ((lane >> 4) & 1) + (lane >> 5)) * NWG + b) * 128 + (lane & 7) * 16;
    const f32x2 f0 = fold<0>(acc[0]), f1 = fold<1>(acc[1]), f2 = fold<0>(acc[2]);
    st_pair(rs, po + par * (4 * NWG * 128), f2u(par ? f1[0] : f0[0]), f2u(par ? f1[1] : f0[1]), T0, T1);
    if (!par) st_pair(rs, po + 2 * (4 * NWG * 128), f2u(f2[0]), f2u(f2[1]), T0, T1);
    lds_signal(L + L_FLAG + F_PUB, lane);
    if (cw == 1) STAMP(11);
  }
  asm volatile(".Lfk_e2:");
  FLUSH();
}

extern "C" void kernel_launch(void* const* d_in, const int* in_sizes, int n_in, void* d_out, int out_size, void* d_ws, size_t ws_size,
                              hipStream_t stream) {
  fused_kernel<<<NWG, NTHR, 0, stream>>>((const u16*)d_in[0], (const u16*)d_in[1], (const u16*)d_in[2], (const u16*)d_in[3],
                                         (const u16*)d_in[4], (const u16*)d_in[6], (const u16*)d_in[7], (const u16*)d_in[8],
                                         (u16*)d_out, (unsigned char*)d_ws);
  tiny_post<<<1, 64, 0, stream>>>((unsigned char*)d_ws, 1);
}
```

```cpp
#include <hip/hip_runtime.h>
#include <stdint.h>

#define CPRIO 3
#define APRIO 2
#define DLT 62
#define DLT2 DLT
#define DLT3 DLT2
#define KV0 30
#define QK0 17
#define PVLAG 3
#define PVMAX 4
#define WIN 4
#define G0 6
#define TPRE 4
#define VGAP 27
#define SB VGAP
#define TH 1050
#define TML 50
#define TQ 450
#define RDEL 50
#define NR 2
#define RSP 25
#define PDEL 30
#define NP 3
#define PSP 16
#define ANC 0
#define TAL 300
#define AOFF 0
#define ARATE 2
#define A0S 10
#define DRY 0
#define KVP 0
#define XS 4
#define PP 4
#define TLIN 0
#define TRACE 0
#define DBG 0

#define GAS __attribute__((address_space(1)))
#define LAS __attribute__((address_space(3)))
typedef unsigned u32;
typedef unsigned long long u64;
typedef unsigned short u16;
typedef u32 v4u __attribute__((ext_vector_type(4)));
typedef u32 v2u __attribute__((ext_vector_type(2)));
typedef float f32x4 __attribute__((ext_vector_type(4)));
typedef float f32x2 __attribute__((ext_vector_type(2)));
typedef __bf16 bf16x8 __attribute__((ext_vector_type(8)));
typedef __bf16 bf16x2 __attribute__((ext_vector_type(2)));
typedef __amdgpu_buffer_rsrc_t rsrc_t;

constexpr int D = 576, SK = 8192, DH = 128, FF = 4608, QD = 4096;
constexpr int NWG = 256, NTHR = 768;
constexpr int NRED = 288 / PP, RED0 = NWG - NRED;
constexpr int GKS = 3, NGU = 72 * GKS;
constexpr int WS_EPOCH = 0;
constexpr int WS_ANC = 2048;
constexpr int WS_Q = 4096;
constexpr int WS_REC = WS_Q + 16 * 4 * 128 * 16;
constexpr int WS_H = WS_REC + NWG * 16 * 8 * 16;
constexpr int WS_ML = WS_H + GKS * FF * 16;
constexpr int WS_PART = WS_ML + NWG * 128;
constexpr int WS_END = WS_PART + NRED * NWG * PP * 16;
constexpr int WS_DBG = 4 << 20;
constexpr u32 SPIN_CAP = 1u << 15;
constexpr int OOB = 0x40000000;
constexpr int L_FLAG = 0;
constexpr int L_SC = 16;
constexpr int L_TAG = 24;
constexpr int L_TA = 26;
constexpr int L_OV = 32;
constexpr int L_Q = 64;
constexpr int L_MX = L_Q + 128;
constexpr int L_LS = L_MX + 16;
constexpr int L_ORED = L_LS + 16;
constexpr int L_XT = L_ORED + 9 * 256;
constexpr int L_QP = L_XT + 640;
constexpr int L_GU = L_QP + 512;
constexpr int L_FIN = L_GU + 512;
constexpr int L_HV = L_FIN + 144;
constexpr int L_WORDS = L_HV + 32;
enum { F_QRDY = 0, F_MAXC = 1, F_GU = 2, F_XN = 3, F_QP = 4, F_LS = 5, F_H = 6, F_DONE = 8, F_FIN = 9, F_PUB = 10, F_DRY = 4   };

__device__ __forceinline__ float u2f(u32 x) { return __builtin_bit_cast(float, x); }
__device__ __forceinline__ u32 f2u(float x) { return __builtin_bit_cast(u32, x); }
__device__ __forceinline__ float bf_lo(u32 x) { return u2f(x << 16); }
__device__ __forceinline__ float bf_hi(u32 x) { return u2f(x & 0xffff0000u); }
__device__ __forceinline__ u32 cvtpk(float lo, float hi) {
  f32x2 v = {lo, hi};
  return __builtin_bit_cast(u32, __builtin_convertvector(v, bf16x2));
}
__device__ __forceinline__ float rbf(float x) { return (float)(__bf16)x; }
__device__ __forceinline__ float rcp(float x) { return __builtin_amdgcn_rcpf(x); }
__device__ __forceinline__ float opq(float x) { asm volatile("" : "+v"(x)); return x; }
__device__ __forceinline__ int opqs(int x) { asm volatile("" : "+s"(x)); return x; }
template <int CTRL> __device__ __forceinline__ u32 dppu(u32 x) { return (u32)__builtin_amdgcn_mov_dpp((int)x, CTRL, 0xf, 0xf, true); }
template <int CTRL> __device__ __forceinline__ float dpp(float x) { return u2f(dppu<CTRL>(f2u(x))); }
constexpr int XOR1 = 0xB1, XOR2 = 0x4E, HMIR = 0x141, XOR8 = 0x128;
__device__ __forceinline__ float xrow_max(float x) {
  auto s = __builtin_amdgcn_permlane16_swap(f2u(x), f2u(x), false, false);
  x = fmaxf(u2f(s[0]), u2f(s[1]));
  auto t = __builtin_amdgcn_permlane32_swap(f2u(x), f2u(x), false, false);
  return fmaxf(u2f(t[0]), u2f(t[1]));
}
__device__ __forceinline__ float xrow_sum(float x) {
  auto s = __builtin_amdgcn_permlane16_swap(f2u(x), f2u(x), false, false);
  x = u2f(s[0]) + u2f(s[1]);
  auto t = __builtin_amdgcn_permlane32_swap(f2u(x), f2u(x), false, false);
  return u2f(t[0]) + u2f(t[1]);
}
__device__ __forceinline__ float row_sum(float x) {
  x = opq(x);
  x += dpp<XOR1>(x); x += dpp<XOR2>(x); x += dpp<HMIR>(x); x += dpp<XOR8>(x); return x;
}
__device__ __forceinline__ f32x4 mfma(v4u a, v4u b, f32x4 c) {
  return __builtin_amdgcn_mfma_f32_16x16x32_bf16(__builtin_bit_cast(bf16x8, a), __builtin_bit_cast(bf16x8, b), c, 0, 0, 0);
}
template <int ODD> __device__ __forceinline__ f32x2 fold(f32x4 a) {
  auto s0 = __builtin_amdgcn_permlane32_swap(f2u(a[0]), f2u(a[2]), false, false);
  auto s1 = __builtin_amdgcn_permlane32_swap(f2u(a[1]), f2u(a[3]), false, false);
  f32x2 r;
  if (ODD) { r[0] = dpp<XOR8>(u2f(s0[0])) + u2f(s0[1]); r[1] = dpp<XOR8>(u2f(s1[0])) + u2f(s1[1]); }
  else { r[0] = u2f(s0[0]) + dpp<XOR8>(u2f(s0[1])); r[1] = u2f(s1[0]) + dpp<XOR8>(u2f(s1[1])); }
  return r;
}
#define LGKM0() asm volatile("s_waitcnt lgkmcnt(0)" ::: "memory")
#define SCHEDB() __builtin_amdgcn_sched_barrier(0)
template <int N> __device__ __forceinline__ void vmwait() { asm volatile("s_waitcnt vmcnt(%0)" ::"i"(N) : "memory"); }
__device__ __forceinline__ u32 lds_ld(const LAS u32* p) { return __hip_atomic_load(p, __ATOMIC_RELAXED, __HIP_MEMORY_SCOPE_WORKGROUP); }
__device__ __forceinline__ void lds_wait_ge(LAS u32* p, u32 n) {
#pragma unroll 1
  for (u32 i = 0; i < (1u << 20); ++i) {
    if ((u32)__builtin_amdgcn_readfirstlane(lds_ld(p)) >= n) break;
    __builtin_amdgcn_s_sleep(1);
  }
  asm volatile("" ::: "memory");
}
__device__ __forceinline__ u32 lds_add(LAS u32* p, int lane) {
  asm volatile("" ::: "memory");
  u32 r = 0;
  if (lane == 0) r = __hip_atomic_fetch_add(p, 1u, __ATOMIC_RELAXED, __HIP_MEMORY_SCOPE_WORKGROUP);
  asm volatile("" ::: "memory");
  return r;
}
__device__ __forceinline__ u32 lds_add_nw(LAS u32* p) {
  u32 r;
  asm volatile("s_mov_b64 exec, 1\n\tds_add_rtn_u32 %0, %1, %2\n\ts_mov_b64 exec, -1" : "=v"(r) : "v"((u32)(uintptr_t)p), "v"(1u) : "memory");
  return r;
}
__device__ __forceinline__ u32 lds_arrive(LAS u32* p, int lane) {
  u32 r = __builtin_amdgcn_readfirstlane(lds_add(p, lane));
  asm volatile("" ::: "memory");
  return r;
}
__device__ __forceinline__ void lds_signal(LAS u32* p, int lane) {
  asm volatile("" ::: "memory");
  if (lane == 0) __hip_atomic_fetch_add(p, 1u, __ATOMIC_RELAXED, __HIP_MEMORY_SCOPE_WORKGROUP);
  asm volatile("" ::: "memory");
}
__device__ __forceinline__ u32 now() { return (u32)__builtin_amdgcn_s_memrealtime(); }
template <int SL> __device__ __forceinline__ void wait_sl(u32 t) {
  asm volatile("s_branch .Lwt%=\n"
               ".Lws%=:\n\ts_sleep %1\n"
               ".Lwt%=:\n\ts_memrealtime vcc\n\ts_waitcnt lgkmcnt(0)\n\ts_sub_i32 vcc_hi, %0, vcc_lo\n\ts_cmp_gt_i32 vcc_hi, 0\n\ts_cbranch_scc1 .Lws%="
               : : "s"(t), "i"(SL) : "memory", "scc", "vcc");
}
template <int R> __device__ __forceinline__ int sclamp(int d) {
  asm("s_max_i32 %0, %0, %1\n\ts_min_i32 %0, %0, %2" : "+s"(d) : "i"(-R), "i"(R) : "scc");
  return d;
}
__device__ __forceinline__ void wait_until(u32 t) { wait_sl<1>(t); }
__device__ __forceinline__ void wait_long(u32 t) { wait_sl<12>(t - 45); wait_sl<1>(t); }
template <int I> struct IC { static constexpr int value = I; };
template <int I, int N, class F> __device__ __forceinline__ void static_for(F&& f) {
  if constexpr (I < N) { f(IC<I>{}); static_for<I + 1, N>(f); }
}
constexpr int pv_done(int i) {
  if (i < QK0 + 6) return 0;
  if (i >= 32) return 16;
  const int p = pv_done(i - 1), a = i - 16 - PVLAG + 1;
  return a < p ? p : (a < p + PVMAX ? a : p + PVMAX);
}
constexpr int SL16(int n16) { return (n16 * DLT) >> 8; }
constexpr int GSL = 3 * 16 * 27 / 32;
constexpr int tail_pos(int i) { return 3 * (i < 6 ? 16 * i : i < 9 ? 96 + 4 * (i - 6) : 108 + 16 * (i - 9)); }
constexpr int kv_pos(int i) {
  const int g = i - G0 < 0 ? 0 : (i - G0 > 16 ? 16 : i - G0);
  return 128 * i + GSL * g + (i >= VGAP ? tail_pos(TPRE) : 0);
}
constexpr int PB1 = kv_pos(SB) - (SB == VGAP ? tail_pos(TPRE) : 0), PB2 = kv_pos(32);
constexpr int TT(int p) { return (p < PB1 ? p * DLT : p < PB2 ? PB1 * DLT + (p - PB1) * DLT2 : PB1 * DLT + (PB2 - PB1) * DLT2 + (p - PB2) * DLT3) >> 8; }
constexpr int AUX_SC1 = 16, AUX_NT = 2;
__device__ __forceinline__ rsrc_t mkrs(const void* p, int bytes) { return __builtin_amdgcn_make_buffer_rsrc((void*)p, 0, bytes, 0x00020000); }
__device__ __forceinline__ v4u ld_nt(rsrc_t r, int voff, int soff) { return __builtin_amdgcn_raw_buffer_load_b128(r, voff, soff, AUX_NT); }
__device__ __forceinline__ v4u ld_sc1(rsrc_t rs, int off) { return __builtin_amdgcn_raw_buffer_load_b128(rs, off, 0, AUX_SC1); }
__device__ __forceinline__ void st_pair(rsrc_t rs, int off, u32 a, u32 b, u32 T0, u32 T1) {
  v4u v = {a, T0, b, T1};
  __builtin_amdgcn_raw_buffer_store_b128(v, rs, off, 0, AUX_SC1);
}
__device__ __forceinline__ bool tagged(v4u v, u32 T0, u32 T1) { return v[1] == T0 && v[3] == T1; }
typedef u32 v16u __attribute__((ext_vector_type(16)));
#define GV(p, k, j) "v[48+16*" #p "+4*" #k "+" #j "]"
#define GLD(p, k) "buffer_load_dwordx4 v[48+16*" #p "+4*" #k ":48+16*" #p "+4*" #k "+3], %[a], %[r], 0 offen offset:1024*" #k " sc1\n\t"
#define GML(p, k) "s_orn2_b64 exec, 1, %[m" #k "]\n\t" GLD(p, k)
#define GCK(p, k) "v_cmp_eq_u32 vcc, %[t0], " GV(p, k, 1) "\n\tv_cmp_eq_u32 %[x], %[t1], " GV(p, k, 3) "\n\ts_and_b64 vcc, vcc, %[x]\n\t" \
                  "v_cndmask_b32 %[a" #k "], %[a" #k "], " GV(p, k, 0) ", vcc\n\tv_cndmask_b32 %[b" #k "], %[b" #k "], " GV(p, k, 2) ", vcc\n\t" \
                  "s_or_b64 %[m" #k "], %[m" #k "], vcc\n\t"
#define GST1(p) "s_waitcnt vmcnt(%[w])\n\t" GCK(p, 0) "s_cmp_eq_u64 %[m0], -1\n\ts_cbranch_scc1 .Lge%=\n\t" GML(p, 0) "s_mov_b64 exec, -1\n\t"
#define GST2(p) "s_waitcnt vmcnt(%[w])\n\t" GCK(p, 0) GCK(p, 1) "s_and_b64 %[x], %[m0], %[m1]\n\ts_cmp_eq_u64 %[x], -1\n\ts_cbranch_scc1 .Lge%=\n\t" \
                GML(p, 0) GML(p, 1) "s_mov_b64 exec, -1\n\t"
#define GST4(p) "s_waitcnt vmcnt(%[w])\n\t" GCK(p, 0) GCK(p, 1) GCK(p, 2) GCK(p, 3) \
                "s_and_b64 %[x], %[m0], %[m1]\n\ts_and_b64 vcc, %[m2], %[m3]\n\ts_and_b64 %[x], %[x], vcc\n\ts_cmp_eq_u64 %[x], -1\n\ts_cbranch_scc1 .Lge%=\n\t" \
                GML(p, 0) GML(p, 1) GML(p, 2) GML(p, 3) "s_mov_b64 exec, -1\n\t"
#define GEND "s_sub_u32 %[n], %[n], 1\n\ts_cbranch_scc0 .Lgl%=\n.Lge%=:"
struct Polls { v16u g0, g1, g2, g3; };
template <int N, int R, int SP> __device__ __forceinline__ void gather(float (&pa)[4], float (&pb)[4], Polls& g, v4u rs, int off, u32 tp, u32 cap, u32 T0, u32 T1) {
  static_assert((N == 1 || N == 2 || N == 4) && R >= 1 && R <= 4, "");
#define G1ST(p, reg, var) \
  if constexpr (p < R) { \
    wait_until(tp + p * SP); \
    if constexpr (N == 1) asm volatile(GLD(p, 0) : "=&{" reg "}"(var) : [a] "v"(off), [r] "s"(rs) : "memory"); \
    else if constexpr (N == 2) asm volatile(GLD(p, 0) GLD(p, 1) : "=&{" reg "}"(var) : [a] "v"(off), [r] "s"(rs) : "memory"); \
    else asm volatile(GLD(p, 0) GLD(p, 1) GLD(p, 2) GLD(p, 3) : "=&{" reg "}"(var) : [a] "v"(off), [r] "s"(rs) : "memory"); \
  } else asm volatile("" : "={" reg "}"(var));
  G1ST(0, "v[48:63]", g.g0) G1ST(1, "v[64:79]", g.g1) G1ST(2, "v[80:95]", g.g2) G1ST(3, "v[96:111]", g.g3)
  u64 m0 = 0, m1 = 0, m2 = 0, m3 = 0, x;
#define GRUN(text) \
  asm volatile(".Lgl%=:\n\t" text GEND \
               : "+{v[48:63]}"(g.g0), "+{v[64:79]}"(g.g1), "+{v[80:95]}"(g.g2), "+{v[96:111]}"(g.g3), [a0] "=&v"(pa[0]), [b0] "=&v"(pb[0]), [a1] "=&v"(pa[1]), \
                 [b1] "=&v"(pb[1]), [a2] "=&v"(pa[2]), [b2] "=&v"(pb[2]), [a3] "=&v"(pa[3]), [b3] "=&v"(pb[3]), [m0] "+s"(m0), [m1] "+s"(m1), [m2] "+s"(m2), \
                 [m3] "+s"(m3), [x] "=&s"(x), [n] "+s"(cap) \
               : [a] "v"(off), [r] "s"(rs), [t0] "s"(T0), [t1] "s"(T1), [w] "i"(N * (R - 1)) : "memory", "scc", "vcc")
  if constexpr (N == 1) {
    if constexpr (R == 1) GRUN(GST1(0));
    if constexpr (R == 2) GRUN(GST1(0) GST1(1));
    if constexpr (R == 3) GRUN(GST1(0) GST1(1) GST1(2));
    if constexpr (R == 4) GRUN(GST1(0) GST1(1) GST1(2) GST1(3));
  } else if constexpr (N == 2) {
    if constexpr (R == 1) GRUN(GST2(0));
    if constexpr (R == 2) GRUN(GST2(0) GST2(1));
    if constexpr (R == 3) GRUN(GST2(0) GST2(1) GST2(2));
    if constexpr (R == 4) GRUN(GST2(0) GST2(1) GST2(2) GST2(3));
  } else {
    if constexpr (R == 1) GRUN(GST4(0));
    if constexpr (R == 2) GRUN(GST4(0) GST4(1));
    if constexpr (R == 3) GRUN(GST4(0) GST4(1) GST4(2));
    if constexpr (R == 4) GRUN(GST4(0) GST4(1) GST4(2) GST4(3));
  }
}
__device__ __forceinline__ void keep(Polls& g) {
  asm volatile("s_waitcnt vmcnt(0)" ::"{v[48:63]}"(g.g0), "{v[64:79]}"(g.g1), "{v[80:95]}"(g.g2), "{v[96:111]}"(g.g3) : "memory");
}
#define STAMP(k) do { } while (0)
#define FLUSH() do { } while (0)
#define UNSTAMP() do { } while (0)
#define TRC(i) do { } while (0)
#define TRT(i) do { } while (0)
#define TRFL() do { } while (0)
#define TRW(k) do { } while (0)

__global__ void __launch_bounds__(NTHR, 3) __attribute__((aligned(4096)))
fused_kernel(const u16* __restrict__ x_, const u16* __restrict__ kc_, const u16* __restrict__ vc_, const u16* __restrict__ wq_,
             const u16* __restrict__ wo_, const u16* __restrict__ wg_, const u16* __restrict__ wu_, const u16* __restrict__ wd_,
             u16* __restrict__ out_, unsigned char* ws_) {
  __shared__ __attribute__((aligned(16))) u32 lds_raw[L_WORDS];
  LAS u32* L = (LAS u32*)lds_raw;
  u32 cpf;
  {
    u64 pc; u32 len;
    asm volatile("s_getpc_b64 %0\n.Lfk_pc:\n\ts_mov_b32 %1, .Lfk_e0-.Lfk_pc\n\ts_max_i32 %1, %1, .Lfk_e1-.Lfk_pc\n\ts_max_i32 %1, %1, .Lfk_e2-.Lfk_pc"
                 : "=s"(pc), "=s"(len) : : "scc");
    cpf = __builtin_amdgcn_raw_buffer_load_b32(__builtin_amdgcn_make_buffer_rsrc((void*)pc, 0, len, 0x00020000), threadIdx.x * 64, 0, 0);
    SCHEDB();
  }
  const int b = blockIdx.x;
  const int lane = threadIdx.x & 63;
  const int wave = __builtin_amdgcn_readfirstlane(threadIdx.x >> 6);
  const int h = (b & 7) * 2 + ((b >> 3) & 1), s = b >> 4;
  const int cw = wave - 8;
  asm volatile("" ::"s"(x_), "s"(kc_), "s"(vc_), "s"(wq_), "s"(wo_), "s"(wg_), "s"(wu_), "s"(wd_), "s"(out_), "s"(ws_));
  const rsrc_t rs = mkrs(ws_, WS_END);
  const v4u rsv = {(u32)(uintptr_t)ws_, (u32)((uintptr_t)ws_ >> 32) & 0xffffu, (u32)WS_END, 0x00020000u};
  v4u W0[6], xa, xb;
  v2u ep;
  const size_t kvbase = (size_t)h * (SK * DH * 2);
  constexpr int KST = KVP == 0 ? 8192 : KVP == 1 ? 2048 : 16384;
  const int pb0 = (KVP == 0 ? s * 128 + wave : KVP == 1 ? s * 128 + (wave & 3) * 32 + (wave >> 2) : (s >> 1) * 256 + (s & 1) * 8 + wave) * 1024;
  const rsrc_t rk = mkrs((const char*)kc_ + kvbase, SK * DH * 2), rv = mkrs((const char*)vc_ + kvbase, SK * DH * 2);
  const int ko = pb0 + (((lane >> 2) & 3) * 16 + (lane >> 4) * 4 + (lane & 3)) * 16;
  const int r0 = 144 * (s & 3) + 48 * (cw - 1);
  {
    const rsrc_t rq = mkrs(wave < 8 ? (const char*)kc_ + kvbase : (const char*)wq_, wave < 8 ? SK * DH * 2 : wave == 8 ? 0 : D * QD * 2);
    const int qo = (r0 + (lane >> 3)) * (QD * 2) + (256 * h + 64 * (s >> 2)) * 2 + (lane & 7) * 16;
    W0[0] = ld_nt(rq, wave < 8 ? ko : qo, 0);
    if (wave >= 8) {
      const rsrc_t rx = mkrs(x_, wave == 8 ? D * 2 : 0), re = mkrs(ws_, wave == 8 ? 8 : 0);
      xa = __builtin_amdgcn_raw_buffer_load_b128(rx, lane * 16, 0, 0);
      xb = __builtin_amdgcn_raw_buffer_load_b128(rx, 1024 + (lane & 7) * 16, 0, 0);
      ep = __builtin_amdgcn_raw_buffer_load_b64(re, WS_EPOCH, 0, AUX_SC1);
#pragma unroll
      for (int i = 1; i < 6; ++i) W0[i] = ld_nt(rq, qo, i * 8 * QD * 2);
    }
  }
  SCHEDB();
  const u32 t0 = now();
  if (wave == 8) {
    u32 iv = (lane >> 4) == 2 || lane == L_SC + 6 ? 1u : 0u;
    if (lane == F_DONE + F_DRY || lane == F_FIN + F_DRY) iv = 7u;
    if (lane == L_TA) iv = t0;
    L[lane] = iv;
    LGKM0();
  }
  v4u mk;
  {
    const int e = lane & 7;
    const u32 hv = (e & 1) ? 0xffff0000u : 0x0000ffffu;
    mk[0] = (e >> 1) == 0 ? hv : 0u; mk[1] = (e >> 1) == 1 ? hv : 0u; mk[2] = (e >> 1) == 2 ? hv : 0u; mk[3] = (e >> 1) == 3 ? hv : 0u;
  }
  const int par = (lane >> 3) & 1;
  LAS u32* xt = L + L_XT;
  __builtin_amdgcn_s_barrier();
  u32 T0 = 0, T1 = 0;

  if (wave == 8) {
    __builtin_amdgcn_s_setprio(CPRIO);
    float v[16];
#pragma unroll
    for (int k = 0; k < 4; ++k) { v[2 * k] = bf_lo(xa[k]); v[2 * k + 1] = bf_hi(xa[k]); v[8 + 2 * k] = bf_lo(xb[k]); v[9 + 2 * k] = bf_hi(xb[k]); }
    const bool tail = lane < 8;
    float s0 = ((v[0] + v[1]) + (v[2] + v[3])) + ((v[4] + v[5]) + (v[6] + v[7]));
    float s1 = ((v[8] + v[9]) + (v[10] + v[11])) + ((v[12] + v[13]) + (v[14] + v[15]));
    const float mean = xrow_sum(row_sum(s0 + (tail ? s1 : 0.f))) * (1.f / D);
    float q0 = 0.f, q1 = 0.f;
#pragma unroll
    for (int k = 0; k < 8; ++k) { float d0 = v[k] - mean, d1 = v[8 + k] - mean; q0 += d0 * d0; q1 += d1 * d1; }
    const float var = xrow_sum(row_sum(q0 + (tail ? q1 : 0.f))) * (1.f / D);
    const float mb = rbf(mean), vb = rbf(var);
    const float rden = rcp(rbf(__builtin_amdgcn_sqrtf(rbf(vb + 1e-5f))));
#pragma unroll
    for (int k = 0; k < 8; ++k) {
      const float a = rbf(v[k] - mb) * rden;
      xt[k * 80 + lane] = cvtpk(a, a);
    }
    if (tail) {
#pragma unroll
      for (int k = 0; k < 8; ++k) {
        const float a = rbf(v[8 + k] - mb) * rden;
        xt[k * 80 + 64 + lane] = cvtpk(a, a);
      }
    }
    const u64 e1 = (((u64)(u32)__builtin_amdgcn_readfirstlane(ep[1]) << 32) | (u32)__builtin_amdgcn_readfirstlane(ep[0])) + 1;
    T0 = (u32)e1;
    T1 = (u32)(e1 >> 32) + T0 * 0x9E3779B1u;
    if (lane == 0) *(LAS v2u*)(L + L_TAG) = v2u{T0, T1};
    lds_signal(L + L_FLAG + F_XN, lane);
    __builtin_amdgcn_s_setprio(0);
    STAMP(14);
  } else if (wave > 8) {
    __builtin_amdgcn_s_setprio(CPRIO);
    lds_wait_ge(L + L_FLAG + F_XN, 1);
    const LAS u32* xr = xt + (lane >> 3) * 80 + (r0 >> 3);
    const v2u tg = *(const LAS v2u*)(L + L_TAG);
    f32x4 a0 = {0.f, 0.f, 0.f, 0.f}, a1 = {0.f, 0.f, 0.f, 0.f};
#pragma unroll
    for (int i = 0; i < 6; i += 2) {
      const v2u xv = *(const LAS v2u*)(xr + i);
      a0 = mfma(xv[0] & mk, W0[i], a0);
      a1 = mfma(xv[1] & mk, W0[i + 1], a1);
    }
    a0 += a1;
    T0 = __builtin_amdgcn_readfirstlane(tg[0]);
    T1 = __builtin_amdgcn_readfirstlane(tg[1]);
    if ((lane >> 5) == par) {
      LAS u32* g = L + L_QP + (8 * (lane & 7) + 4 * ((lane >> 4) & 1)) * 8 + (cw - 1) * 2 + par;
#pragma unroll
      for (int r = 0; r < 4; ++r) g[r * 8] = f2u(a0[r]);
    }
    const u32 old = lds_arrive(L + L_FLAG + F_QP, lane);
    if (old == 2) {
      const LAS u32* qp = L + L_QP + (lane & 31) * 16;
      const f32x4 a = *(const LAS f32x4*)qp, a2 = *(const LAS f32x4*)(qp + 8);
      const f32x2 c = *(const LAS f32x2*)(qp + 4), c2 = *(const LAS f32x2*)(qp + 12);
      const float q = ((a[0] + a[1]) + (a[2] + a[3])) + (c[0] + c[1]);
      const float qn = ((a2[0] + a2[1]) + (a2[2] + a2[3])) + (c2[0] + c2[1]);
      if (lane < 32) st_pair(rs, WS_Q + ((h * 4 + (s & 3)) * 128 + 32 * (s >> 2) + lane) * 16, f2u(q), f2u(qn), T0, T1);
      STAMP(5);
    }
  }
  const int dry = wave == 8 ? 1 : 0;
  const int dz = dry ? OOB : 0;
  const u32 cap = dry ? 1u : SPIN_CAP;

  if (wave < 8 + DRY) {
    f32x4 acc0 = {0.f, 0.f, 0.f, 0.f};
    int rec = dz + WS_REC + (((h * 16 + (lane >> 3)) * 16 + s) * 8 + (lane & 7)) * 16, rec2 = rec + 8 * 2048;
    asm volatile("" : "+v"(rec), "+v"(rec2));
    if (wave < 8) {
      v4u K[16], V[16];
      K[0] = W0[0];
      const int vo = pb0 + lane * 16;
      const u32 tw0 = KV0 + ((wave * DLT) >> 4);
      u32 tb = t0 + tw0, tav = 0;
      const int hb = par;
      const bool t1 = (lane >> 2) & 1;
      v4u Be, Bo;
      float sc[8];
      u32 pk[8];
      float m = -INFINITY, ls = 0.f;
      f32x4 acc1 = {0.f, 0.f, 0.f, 0.f};
      __builtin_amdgcn_s_setprio(APRIO);
      static_for<1, 33>([&](auto ic) __attribute__((always_inline)) {
        constexpr int i = decltype(ic)::value;
        if constexpr (i < 32) {
          wait_until(tb + TT(kv_pos(i)));
          TRC(i);
          if constexpr (i > WIN) vmwait<WIN>();
          if constexpr (i == 1) V[0] = ld_nt(rv, vo, 0);
          else if constexpr (i < 17) K[i - 1] = ld_nt(rk, ko, (i - 1) * KST);
          else V[i - 16] = ld_nt(rv, vo, (i - 16) * KST);
          SCHEDB();
          TRT(i);
          SCHEDB();
        }
        if constexpr (i == WIN + 1) asm volatile("" ::"v"(cpf));
        if (i == 31 && wave == 7) STAMP(1);
        if constexpr (i == QK0) {
          lds_wait_ge(L + L_FLAG + F_QRDY, 1);
          if (wave == 0) STAMP(2);
          const v4u qf = *(const LAS v4u*)(L + L_Q + hb * 64 + (lane >> 4) * 16 + (lane & 3) * 4);
          const v4u zero4 = {0u, 0u, 0u, 0u};
          Be = t1 ? zero4 : qf; Bo = t1 ? qf : zero4;
        }
        if constexpr (i >= QK0 && i < QK0 + 4) {
#pragma unroll
          for (int j = 2 * (i - QK0); j < 2 * (i - QK0) + 2; ++j) {
            f32x4 d = {0.f, 0.f, 0.f, 0.f};
            d = mfma(K[2 * j], Be, d);
            d = mfma(K[2 * j + 1], Bo, d);
            float v = (dpp<0x00>(d[0]) + dpp<0x55>(d[1])) + (dpp<0xAA>(d[2]) + dpp<0xFF>(d[3]));
            v = rbf(rbf(v) * 0.08838834764831845f);
            sc[j] = v;
            m = fmaxf(m, v);
          }
        }
        if constexpr (i == QK0 + 3) {
          m = fmaxf(m, dpp<HMIR>(m));
          m = xrow_max(m);
          if ((lane & 55) == 0) L[L_MX + hb * 8 + wave] = f2u(m);
          lds_signal(L + L_FLAG + F_MAXC, lane);
        }
        if constexpr (i == QK0 + 5) {
          lds_wait_ge(L + L_FLAG + F_MAXC, 8);
          const f32x4 a = *(const LAS f32x4*)(L + L_MX + hb * 8), c = *(const LAS f32x4*)(L + L_MX + hb * 8 + 4);
          m = fmaxf(fmaxf(fmaxf(a[0], a[1]), fmaxf(a[2], a[3])), fmaxf(fmaxf(c[0], c[1]), fmaxf(c[2], c[3])));
        }
        if constexpr (i == QK0 + 5 || i == QK0 + 6) {
#pragma unroll
          for (int j = (i == QK0 + 5 ? 0 : XS); j < (i == QK0 + 5 ? XS : 8); ++j) {
            const float p = __expf(sc[j] - m);
            ls += p;
            pk[j] = cvtpk(p, p);
          }
        }
        if constexpr (i == QK0 + 6) {
          ls += dpp<HMIR>(ls);
          ls = xrow_sum(ls);
          if ((lane & 55) == 0) L[L_LS + hb * 8 + wave] = f2u(ls);
          lds_signal(L + L_FLAG + F_LS, lane);
          if (wave == 0) STAMP(3);
        }
        if constexpr (i == QK0 + 4) {
          const v2u tg = *(const LAS v2u*)(L + L_TAG);
          T0 = __builtin_amdgcn_readfirstlane(tg[0]); T1 = __builtin_amdgcn_readfirstlane(tg[1]);
        }
        if constexpr (i >= QK0 + 6) {
          constexpr int lo = pv_done(i - 1), hi = pv_done(i);
#pragma unroll
          for (int j = lo; j < hi; ++j) {
            const u32 pp = pk[j >> 1], pm = dppu<HMIR>(pp);
            if (j & 1) acc1 = mfma(v4u{pm & mk[0], pm & mk[1], pp & mk[2], pp & mk[3]}, V[j], acc1);
            else acc0 = mfma(v4u{pp & mk[0], pp & mk[1], pm & mk[2], pm & mk[3]}, V[j], acc0);
          }
        }
        SCHEDB();
      });
      acc0 += acc1;
    }
    *(LAS f32x4*)(L + L_ORED + wave * 256 + (lane >> 5) * 128 + 8 * (lane & 15) + 4 * ((lane >> 4) & 1)) = acc0;
    if (wave == 7) STAMP(15);
    TRT(32);
    {
      u32 old = lds_add_nw(L + L_FLAG + F_DONE + dry * F_DRY);
      f32x2 r[2][8];
#pragma unroll
      for (int k = 0; k < 8; ++k) {
        r[0][k] = *(const LAS f32x2*)(L + L_ORED + k * 256 + 2 * lane);
        r[1][k] = *(const LAS f32x2*)(L + L_ORED + k * 256 + 128 + 2 * lane);
      }
      const volatile LAS v4u* fp = (const volatile LAS v4u*)(L + L_SC + 4 * dry);
      v4u fs = *fp;
      f32x2 o[2];
#pragma unroll
      for (int hh = 0; hh < 2; ++hh)
        o[hh] = ((r[hh][0] + r[hh][1]) + (r[hh][2] + r[hh][3])) + ((r[hh][4] + r[hh][5]) + (r[hh][6] + r[hh][7]));
      asm volatile("" : "+v"(old), "+v"(o[0]), "+v"(o[1]), "+v"(fs));
      old = __builtin_amdgcn_readfirstlane(old);
      if (old == 7) {
        STAMP(16);
#pragma unroll 1
        for (u32 i = 0; i < (1u << 20); ++i) {
          if (__builtin_amdgcn_readfirstlane(fs[2])) break;
          __builtin_amdgcn_s_sleep(1);
          fs = *fp;
        }
        const u32 f0 = fs[0], f1 = fs[1];
        st_pair(rs, rec, f2u(o[0][0] * u2f(f0)), f2u(o[0][1] * u2f(f0)), T0, T1);
        st_pair(rs, rec2, f2u(o[1][0] * u2f(f1)), f2u(o[1][1] * u2f(f1)), T0, T1);
        STAMP(4);
      }
    }
    if (b >= RED0) {
      const int r = b - RED0, rr = r / (8 / PP), fl = lane & (2 * PP - 1);
      const int col = 64 * (rr >> 2) + 4 * ((rr >> 1) & 1) + 2 * (rr & 1) + 8 * ((r % (8 / PP)) * PP + (fl >> 1)) + (lane & 1);
      u32 xo = x_[col];
      const rsrc_t rout = mkrs(out_, D * 2);
      const int pb = dz + WS_PART + (r * NWG + wave * 32) * (PP * 16) + lane * 16;
      Polls gp;
      float ga[4], gb[4];
      lds_wait_ge(L + L_FLAG + F_PUB, dry ? 0 : 3);
      asm volatile("" : "+v"(xo));
      if (wave == 0) STAMP(12);
      gather<PP / 2, NP, PSP>(ga, gb, gp, rsv, pb, now() + (dry ? -1000 : PDEL), cap, T0, T1);
      if (wave == 0) STAMP(23);
      float a0 = PP == 8 ? (ga[0] + ga[1]) + (ga[2] + ga[3]) : PP == 4 ? ga[0] + ga[1] : ga[0];
      float a1 = PP == 8 ? (gb[0] + gb[1]) + (gb[2] + gb[3]) : PP == 4 ? gb[0] + gb[1] : gb[0];
      a0 += dpp<XOR8>(a0); a1 += dpp<XOR8>(a1);
      if (PP < 8) { a0 += dpp<0x124>(a0); a1 += dpp<0x124>(a1); }
      if (PP < 4) { a0 += dpp<0x122>(a0); a1 += dpp<0x122>(a1); }
      a0 = xrow_sum(a0); a1 = xrow_sum(a1);
      if (lane < PP) *(LAS v2u*)(L + L_FIN + wave * 16 + 2 * lane) = v2u{f2u(a0), f2u(a1)};
      u32 old2 = lds_add_nw(L + L_FLAG + F_FIN + dry * F_DRY);
      const LAS float* f = (const LAS float*)(L + L_FIN) + fl;
      const float A = ((f[0] + f[16]) + (f[32] + f[48])) + ((f[64] + f[80]) + (f[96] + f[112]));
      float o = bf_lo(xo) + A;
      asm volatile("" : "+v"(old2), "+v"(o));
      old2 = __builtin_amdgcn_readfirstlane(old2);
      if (old2 == 7) {
        __builtin_amdgcn_raw_buffer_store_b16((u16)cvtpk(o, o), rout, lane < 2 * PP ? dz + col * 2 : OOB, 0, 0);
        const v2u e1 = {T0, T1 - T0 * 0x9E3779B1u};
        __builtin_amdgcn_raw_buffer_store_b64(e1, rs, r == 0 && lane == 0 ? dz + WS_EPOCH : OOB, 0, AUX_SC1);
        STAMP(13);
        asm volatile(".Lfk_e0:");
      }
      keep(gp);
    }
    if (opqs(wave) < 8) {
      FLUSH();
      TRFL();
      return;
    }
    UNSTAMP();
  }

  asm volatile("" ::"v"(cpf));
  const int c = cw - 1;
  if (wave > 8 - DRY) {
    v4u Wo[6];
    f32x4 acc[3];
    const int ct0 = 3 * c;
    if (wave > 8) {
      if (b < NGU) {
        const int gcg = b / GKS, gks = b % GKS;
        const int grp0 = 24 * gks + 8 * c;
        const rsrc_t rg = mkrs(wg_, D * FF * 2), ru = mkrs(wu_, D * FF * 2);
        const int go = (grp0 * 8 + (lane >> 3)) * (FF * 2) + gcg * 128 + (lane & 7) * 16;
        const u32 tg = t0 + KV0 + SL16(128 * G0) + c * SL16((128 + GSL) / 3);
        v4u Wg[16];
#pragma unroll
        for (int i = 0; i < 16; ++i) {
          wait_until(tg + SL16((128 + GSL) * i));
          TRC(1 + i); TRT(1 + i);
          Wg[i] = ld_nt((i & 1) ? ru : rg, go, (i >> 1) * (8 * FF * 2));
          SCHEDB();
        }
        TRW(17);
        const LAS u32* xr = xt + (lane >> 3) * 80 + grp0;
        f32x4 ag = {0.f, 0.f, 0.f, 0.f}, au = {0.f, 0.f, 0.f, 0.f};
#pragma unroll
        for (int i = 0; i < 8; ++i) {
          const v4u A = xr[i] & mk;
          ag = mfma(A, Wg[2 * i], ag);
          au = mfma(A, Wg[2 * i + 1], au);
        }
        const f32x2 fg = fold<0>(ag), fu = fold<1>(au);
        const int cc = 8 * (lane & 7) + 4 * ((lane >> 4) & 1) + 2 * (lane >> 5);
        LAS u32* g = L + L_GU + (par * 64 + cc) * 4 + c;
        g[0] = f2u(par ? fu[0] : fg[0]);
        g[4] = f2u(par ? fu[1] : fg[1]);
        const u32 old = lds_arrive(L + L_FLAG + F_GU, lane);
        if (old == 2) {
          const f32x4 a = *(const LAS f32x4*)(L + L_GU + lane * 4), c4 = *(const LAS f32x4*)(L + L_GU + 256 + lane * 4);
          st_pair(rs, WS_H + (gks * FF + gcg * 64 + lane) * 16, f2u((a[0] + a[1]) + a[2]), f2u((c4[0] + c4[1]) + c4[2]), T0, T1);
          STAMP(8);
        }
      }
      const rsrc_t rd = mkrs(wd_, FF * D * 2), ro = mkrs(wo_, QD * D * 2);
      const int dof = (18 * b + (lane >> 3)) * (D * 2) + ct0 * 128 + (lane & 7) * 16;
      const int dof2 = lane < 16 ? dof : OOB;
      const int oof = (256 * h + 16 * s + (lane >> 3)) * (D * 2) + ct0 * 128 + (lane & 7) * 16;
      v4u Wd[9], Ah[3];
      const u32 ta = t0;
      const u32 tt = ta + KV0 + c * SL16(16);
#pragma unroll
      for (int i = 0; i < 15; ++i) {
        const u32 tw_ = tt + TT((i < TPRE ? kv_pos(VGAP) : kv_pos(32)) - tail_pos(TPRE) + tail_pos(i));
        if (i == 0 || i == TPRE) wait_long(tw_); else wait_until(tw_);
        if (i == TPRE && cw == 1) STAMP(7);
        TRC(32 + i); TRT(32 + i);
        if (i < 9) Wd[i] = ld_nt(rd, i < 6 ? dof : dof2, (i / 3) * 8 * D * 2 + (i % 3) * 128);
        else Wo[i - 9] = ld_nt(ro, oof, ((i - 9) / 3) * 8 * D * 2 + (i % 3) * 128);
        SCHEDB();
      }
      TRW(47);
      lds_wait_ge(L + L_FLAG + F_H, 1);
#pragma unroll
      for (int j = 0; j < 3; ++j) Ah[j] = L[L_HV + 8 * j + (lane >> 3)] & mk;
#pragma unroll
      for (int t = 0; t < 3; ++t) {
        acc[t] = mfma(Ah[0], Wd[t], f32x4{0.f, 0.f, 0.f, 0.f});
        acc[t] = mfma(Ah[1], Wd[3 + t], acc[t]);
        acc[t] = mfma(Ah[2], Wd[6 + t], acc[t]);
      }
      if (cw == 1) STAMP(17);
    } else {
#pragma unroll
      for (int i = 0; i < 6; ++i) asm volatile("" : "=v"(Wo[i]));
#pragma unroll
      for (int i = 0; i < 3; ++i) asm volatile("" : "=v"(acc[i]));
    }
    const int po = (dz | WS_PART) + (((4 * (dry ? 0 : ct0) + 2 * ((lane >> 4) & 1) + (lane >> 5)) * (8 / PP) + (lane & 7) / PP) * NWG + b) * (PP * 16) + (lane & (PP - 1)) * 16;
    const LAS u32* ov = L + L_OV + 16 * dry + (lane >> 3);
    u32 o0, o1;
#pragma unroll 1
    for (u32 i = 0; i < (1u << 20); ++i) {
      o0 = lds_ld(ov); o1 = lds_ld(ov + 8);
      if (__all(o0 != 1u && o1 != 1u)) break;
    }
    const v4u A0 = o0 & mk, A1 = o1 & mk;
    if (cw == 1) STAMP(18);
#pragma unroll
    for (int t = 0; t < 3; ++t) acc[t] = mfma(A0, Wo[t], acc[t]);
#pragma unroll
    for (int t = 0; t < 3; ++t) acc[t] = mfma(A1, Wo[3 + t], acc[t]);
    const f32x2 f0 = fold<0>(acc[0]), f1 = fold<1>(acc[1]), f2 = fold<0>(acc[2]);
    st_pair(rs, po + par * (4 * NWG * 128), f2u(par ? f1[0] : f0[0]), f2u(par ? f1[1] : f0[1]), T0, T1);
    if (!par) st_pair(rs, po + 2 * (4 * NWG * 128), f2u(f2[0]), f2u(f2[1]), T0, T1);
    lds_signal(L + L_FLAG + F_PUB + dry * F_DRY, lane);
    if (cw == 1) STAMP(11);
    asm volatile(".Lfk_e1:");
    if (opqs(wave) > 8) {
      FLUSH();
      TRFL();
      return;
    }
  }

#pragma unroll 1
  for (int pass = 1 - DRY; pass < 2; ++pass) {
    pass = opqs(pass);
    if (pass) {
      STAMP(20);
      __builtin_amdgcn_s_setprio(CPRIO);
      u32 tch;
      {
        const char* p = (lane & 1) ? (const char*)wd_ + (18 * b) * (D * 2) : (const char*)wo_ + (256 * h + 16 * s) * (D * 2);
        if (lane & 2) p = (const char*)out_;
        tch = __hip_atomic_load((const GAS u32*)p, __ATOMIC_RELAXED, __HIP_MEMORY_SCOPE_AGENT);
      }
      wait_long(t0 + TQ);
      asm volatile("" ::"v"(tch));
      v4u g[8];
      bool ok = false;
#pragma unroll 1
      for (u32 it = 0; it < SPIN_CAP; ++it) {
        if (!ok) {
#pragma unroll
          for (int k = 0; k < 8; ++k) g[k] = ld_sc1(rs, WS_Q + (512 * h + 64 * k + lane) * 16);
        }
        ok = true;
#pragma unroll
        for (int k = 0; k < 8; ++k) ok = ok && tagged(g[k], T0, T1);
        if (__all(ok)) break;
        __builtin_amdgcn_s_sleep(16);
        asm volatile("" ::: "memory");
      }
#pragma unroll
      for (int k = 0; k < 2; ++k) {
        const float qa = (u2f(g[k][0]) + u2f(g[2 + k][0])) + (u2f(g[4 + k][0]) + u2f(g[6 + k][0]));
        const float qb = (u2f(g[k][2]) + u2f(g[2 + k][2])) + (u2f(g[4 + k][2]) + u2f(g[6 + k][2]));
        L[L_Q + 64 * k + lane] = cvtpk(qa, qb);
      }
      lds_signal(L + L_FLAG + F_QRDY, lane);
      STAMP(6);
      wait_long(t0 + KV0 + TT(kv_pos(QK0 + 6)) - 60);
      lds_wait_ge(L + L_FLAG + F_LS, 8);
      {
        const LAS u32* sp = L + L_MX + ((lane ^ (lane >> 1)) & 1) * 8;
        const f32x4 a = *(const LAS f32x4*)sp, c4 = *(const LAS f32x4*)(sp + 4), e = *(const LAS f32x4*)(sp + 16), f = *(const LAS f32x4*)(sp + 20);
        const float Mh = fmaxf(fmaxf(fmaxf(a[0], a[1]), fmaxf(a[2], a[3])), fmaxf(fmaxf(c4[0], c4[1]), fmaxf(c4[2], c4[3])));
        const float Lh = ((e[0] + e[1]) + (e[2] + e[3])) + ((f[0] + f[1]) + (f[2] + f[3]));
        if (lane < 8) st_pair(rs, WS_ML + (h * 16 + s) * 128 + lane * 16, f2u(Mh), f2u(Lh), T0, T1);
      }
      STAMP(21);
      wait_long(now() + TML);
      wait_long(t0 + TH);
      const int mo = WS_ML + (h * 16 + (lane >> 2)) * 128 + (lane & 3) * 16;
      const bool act = lane < 18;
      const int ho = act ? WS_H + (18 * b + lane) * 16 : OOB;
      v4u gm = ld_sc1(rs, mo), gh[GKS];
#pragma unroll
      for (int k = 0; k < GKS; ++k) gh[k] = ld_sc1(rs, ho + k * FF * 16);
      SCHEDB();
#pragma unroll 1
      for (u32 it = 0; it < SPIN_CAP; ++it) {
        ok = tagged(gm, T0, T1);
        if (__all(ok)) break;
        __builtin_amdgcn_s_sleep(4);
        asm volatile("" ::: "memory");
        if (!ok) gm = ld_sc1(rs, mo);
      }
      {
        const u32 gm0 = gm[0], gm2 = gm[2];
        float M = fmaxf(u2f(gm0), dpp<HMIR>(u2f(gm0)));
        M = xrow_max(fmaxf(M, dpp<XOR8>(M)));
        const float w = __expf(u2f(gm0) - M);
        float lt = opq(w * u2f(gm2));
        lt += dpp<HMIR>(lt);
        lt += dpp<XOR8>(lt);
        const float fc = w * rcp(xrow_sum(lt));
        const u32 f0 = __builtin_amdgcn_readlane(f2u(fc), 4 * s), f1 = __builtin_amdgcn_readlane(f2u(fc), 4 * s + 1);
        if (lane == 0) *(volatile LAS v4u*)(L + L_SC) = v4u{f0, f1, 1u, 0u};
        STAMP(22);
      }
#pragma unroll 1
      for (u32 it = 0; it < SPIN_CAP; ++it) {
        ok = true;
#pragma unroll
        for (int k = 0; k < GKS; ++k) ok = ok && tagged(gh[k], T0, T1);
        ok = ok || !act;
        if (__all(ok)) break;
        __builtin_amdgcn_s_sleep(8);
        asm volatile("" ::: "memory");
        if (!ok) {
#pragma unroll
          for (int k = 0; k < GKS; ++k) gh[k] = ld_sc1(rs, ho + k * FF * 16);
        }
      }
      float gs = u2f(gh[0][0]), us = u2f(gh[0][2]);
#pragma unroll
      for (int k = 1; k < GKS; ++k) { gs += u2f(gh[k][0]); us += u2f(gh[k][2]); }
      const float gb = rbf(gs), ub = rbf(us);
      float hv = rbf(gb * rcp(1.f + __expf(-gb))) * ub;
      if (!act) hv = 0.f;
      if (lane < 24) L[L_HV + lane] = cvtpk(hv, hv);
      lds_signal(L + L_FLAG + F_H, lane);
      STAMP(9);
      wait_long(t0 + KV0 + TT(kv_pos(32)) - 60);
    }
    lds_wait_ge(L + L_FLAG + F_DONE, pass ? 8 : 0);
    const int off = (pass ? 0 : OOB) + WS_REC + (h * 16 + s) * 2048 + lane * 16;
    Polls rp;
    float ra[4], rb[4];
    STAMP(19);
    gather<2, NR, RSP>(ra, rb, rp, rsv, off, now() + (pass ? RDEL : -1000), pass ? SPIN_CAP : 1u, T0, T1);
    STAMP(10);
    float oa = ra[0] + ra[1], ob = rb[0] + rb[1];
    oa += dpp<XOR8>(oa); ob += dpp<XOR8>(ob);
    oa = xrow_sum(oa); ob = xrow_sum(ob);
    asm volatile(".Lfk_e2:" : "+v"(oa), "+v"(ob));
    if (lane < 8) *(volatile LAS v2u*)(L + L_OV + (pass ? 0 : 16) + 2 * lane) = v2u{cvtpk(oa, oa), cvtpk(ob, ob)};
    keep(rp);
  }
  FLUSH();
}

extern "C" void kernel_launch(void* const* d_in, const int* in_sizes, int n_in, void* d_out, int out_size, void* d_ws, size_t ws_size,
                              hipStream_t stream) {
  fused_kernel<<<NWG, NTHR, 0, stream>>>((const u16*)d_in[0], (const u16*)d_in[1], (const u16*)d_in[2], (const u16*)d_in[3],
                                         (const u16*)d_in[4], (const u16*)d_in[6], (const u16*)d_in[7], (const u16*)d_in[8],
                                         (u16*)d_out, (unsigned char*)d_ws);
}
```

```cpp
#include <hip/hip_runtime.h>
#include <stdint.h>

#define CPRIO 3
#define APRIO 2
#define DLT 62
#define DLT2 58
#define DLT3 55
#define KV0 30
#define QK0 17
#define PVLAG 3
#define PVMAX 4
#define WIN 4
#define G0 6
#define TPRE 0
#define VGAP 27
#define SB 27
#define TH 1050
#define TML 50
#define TQ 450
#define RDEL 45
#define NR 2
#define RSP 25
#define NM 2
#define MSP 40
#define PRE 1
#define TPR 250
#define BACK 0
#define LT 90
#define B0 4
#define BM 0
#define GB 0
#define GP 0
#define TB 0
#define GSLV (3 * 16 * 27 / 32)
#define PDEL 24
#define NP 3
#define PSP 16
#define KVP 0
#define XS 4
#define TRACE 0
#define TOFF 20
#define TD (-25)
#define ROT 0
#define FW 4
#define GATE 1
#define FO 0
#define DBG 0

#define GAS __attribute__((address_space(1)))
#define LAS __attribute__((address_space(3)))
typedef unsigned u32;
typedef unsigned long long u64;
typedef unsigned short u16;
typedef u32 v4u __attribute__((ext_vector_type(4)));
typedef u32 v2u __attribute__((ext_vector_type(2)));
typedef float f32x4 __attribute__((ext_vector_type(4)));
typedef float f32x2 __attribute__((ext_vector_type(2)));
typedef __bf16 bf16x8 __attribute__((ext_vector_type(8)));
typedef __bf16 bf16x2 __attribute__((ext_vector_type(2)));
typedef __amdgpu_buffer_rsrc_t rsrc_t;

constexpr int D = 576, SK = 8192, DH = 128, FF = 4608, QD = 4096;
constexpr int NWG = 256, NTHR = 768;
constexpr int PP = 4;
constexpr int NRED = 288 / PP, RED0 = NWG - NRED;
constexpr int GKS = 3, NGU = 72 * GKS;
constexpr int WS_EPOCH = 0;
constexpr int WS_Q = 4096;
constexpr int WS_REC = WS_Q + 16 * 4 * 128 * 16;
constexpr int WS_H = WS_REC + NWG * 16 * 8 * 16;
constexpr int WS_ML = WS_H + GKS * FF * 16;
constexpr int WS_PART = WS_ML + NWG * 128;
constexpr int WS_END = WS_PART + NRED * NWG * PP * 16;
constexpr int WS_DBG = 4 << 20;
constexpr u32 SPIN_CAP = 1u << 15;
constexpr int OOB = 0x40000000;
constexpr int L_FLAG = 0;
constexpr int L_SC = 16;
constexpr int L_TAG = 24;
constexpr int L_OV = 32;
constexpr int L_Q = 64;
constexpr int L_MX = L_Q + 128;
constexpr int L_LS = L_MX + 16;
constexpr int L_ORED = L_LS + 16;
constexpr int L_XT = L_ORED + 9 * 256;
constexpr int L_QP = L_XT + 640;
constexpr int L_GU = L_QP + 512;
constexpr int L_FIN = L_GU + 512;
constexpr int L_HV = L_FIN + 144;
constexpr int L_PK = L_HV + 32;
constexpr int L_WORDS = L_PK + 512;
enum { F_QRDY = 0, F_MAXC = 1, F_GU = 2, F_XN = 3, F_QP = 4, F_LS = 5, F_H = 6, F_DONE = 8, F_FIN = 9, F_PUB = 10, F_VI = 11 };

__device__ __forceinline__ float u2f(u32 x) { return __builtin_bit_cast(float, x); }
__device__ __forceinline__ u32 f2u(float x) { return __builtin_bit_cast(u32, x); }
__device__ __forceinline__ float bf_lo(u32 x) { return u2f(x << 16); }
__device__ __forceinline__ float bf_hi(u32 x) { return u2f(x & 0xffff0000u); }
__device__ __forceinline__ u32 cvtpk(float lo, float hi) {
  f32x2 v = {lo, hi};
  return __builtin_bit_cast(u32, __builtin_convertvector(v, bf16x2));
}
__device__ __forceinline__ float rbf(float x) { return (float)(__bf16)x; }
__device__ __forceinline__ float rcp(float x) { return __builtin_amdgcn_rcpf(x); }
__device__ __forceinline__ float opq(float x) { asm volatile("" : "+v"(x)); return x; }
template <int CTRL> __device__ __forceinline__ u32 dppu(u32 x) { return (u32)__builtin_amdgcn_mov_dpp((int)x, CTRL, 0xf, 0xf, true); }
template <int CTRL> __device__ __forceinline__ float dpp(float x) { return u2f(dppu<CTRL>(f2u(x))); }
constexpr int XOR1 = 0xB1, XOR2 = 0x4E, HMIR = 0x141, XOR8 = 0x128;
__device__ __forceinline__ float xrow_max(float x) {
  auto s = __builtin_amdgcn_permlane16_swap(f2u(x), f2u(x), false, false);
  x = fmaxf(u2f(s[0]), u2f(s[1]));
  auto t = __builtin_amdgcn_permlane32_swap(f2u(x), f2u(x), false, false);
  return fmaxf(u2f(t[0]), u2f(t[1]));
}
__device__ __forceinline__ float xrow_sum(float x) {
  auto s = __builtin_amdgcn_permlane16_swap(f2u(x), f2u(x), false, false);
  x = u2f(s[0]) + u2f(s[1]);
  auto t = __builtin_amdgcn_permlane32_swap(f2u(x), f2u(x), false, false);
  return u2f(t[0]) + u2f(t[1]);
}
__device__ __forceinline__ float row_sum(float x) {
  x = opq(x);
  x += dpp<XOR1>(x); x += dpp<XOR2>(x); x += dpp<HMIR>(x); x += dpp<XOR8>(x); return x;
}
__device__ __forceinline__ f32x4 mfma(v4u a, v4u b, f32x4 c) {
  return __builtin_amdgcn_mfma_f32_16x16x32_bf16(__builtin_bit_cast(bf16x8, a), __builtin_bit_cast(bf16x8, b), c, 0, 0, 0);
}
template <int ODD> __device__ __forceinline__ f32x2 fold(f32x4 a) {
  auto s0 = __builtin_amdgcn_permlane32_swap(f2u(a[0]), f2u(a[2]), false, false);
  auto s1 = __builtin_amdgcn_permlane32_swap(f2u(a[1]), f2u(a[3]), false, false);
  f32x2 r;
  if (ODD) { r[0] = dpp<XOR8>(u2f(s0[0])) + u2f(s0[1]); r[1] = dpp<XOR8>(u2f(s1[0])) + u2f(s1[1]); }
  else { r[0] = u2f(s0[0]) + dpp<XOR8>(u2f(s0[1])); r[1] = u2f(s1[0]) + dpp<XOR8>(u2f(s1[1])); }
  return r;
}
#define LGKM0() asm volatile("s_waitcnt lgkmcnt(0)" ::: "memory")
#define SCHEDB() __builtin_amdgcn_sched_barrier(0)
template <int N> __device__ __forceinline__ void vmwait() { asm volatile("s_waitcnt vmcnt(%0)" ::"i"(N) : "memory"); }
__device__ __forceinline__ u32 lds_ld(const LAS u32* p) { return __hip_atomic_load(p, __ATOMIC_RELAXED, __HIP_MEMORY_SCOPE_WORKGROUP); }
__device__ __forceinline__ void lds_wait_ge(LAS u32* p, u32 n) {
#pragma unroll 1
  for (u32 i = 0; i < (1u << 20); ++i) {
    if ((u32)__builtin_amdgcn_readfirstlane(lds_ld(p)) >= n) break;
    __builtin_amdgcn_s_sleep(1);
  }
  asm volatile("" ::: "memory");
}
__device__ __forceinline__ u32 lds_add(LAS u32* p, int lane) {
  asm volatile("" ::: "memory");
  u32 r = 0;
  if (lane == 0) r = __hip_atomic_fetch_add(p, 1u, __ATOMIC_RELAXED, __HIP_MEMORY_SCOPE_WORKGROUP);
  asm volatile("" ::: "memory");
  return r;
}
__device__ __forceinline__ u32 lds_add_nw(LAS u32* p) {
  u32 r;
  asm volatile("s_mov_b64 exec, 1\n\tds_add_rtn_u32 %0, %1, %2\n\ts_mov_b64 exec, -1" : "=v"(r) : "v"((u32)(uintptr_t)p), "v"(1u) : "memory");
  return r;
}
__device__ __forceinline__ u32 lds_arrive(LAS u32* p, int lane) {
  u32 r = __builtin_amdgcn_readfirstlane(lds_add(p, lane));
  asm volatile("" ::: "memory");
  return r;
}
__device__ __forceinline__ void lds_signal(LAS u32* p, int lane) {
  asm volatile("" ::: "memory");
  if (lane == 0) __hip_atomic_fetch_add(p, 1u, __ATOMIC_RELAXED, __HIP_MEMORY_SCOPE_WORKGROUP);
  asm volatile("" ::: "memory");
}
__device__ __forceinline__ u32 now() { return (u32)__builtin_amdgcn_s_memrealtime(); }
template <int SL> __device__ __forceinline__ void wait_sl(u32 t) {
  asm volatile("s_branch .Lwt%=\n"
               ".Lws%=:\n\ts_sleep %1\n"
               ".Lwt%=:\n\ts_memrealtime vcc\n\ts_waitcnt lgkmcnt(0)\n\ts_sub_i32 vcc_hi, %0, vcc_lo\n\ts_cmp_gt_i32 vcc_hi, 0\n\ts_cbranch_scc1 .Lws%="
               : : "s"(t), "i"(SL) : "memory", "scc", "vcc");
}
__device__ __forceinline__ void wait_until(u32 t) { wait_sl<1>(t); }
__device__ __forceinline__ void wait_long(u32 t) { wait_sl<12>(t - 45); wait_sl<1>(t); }
template <int I> struct IC { static constexpr int value = I; };
template <int I, int N, class F> __device__ __forceinline__ void static_for(F&& f) {
  if constexpr (I < N) { f(IC<I>{}); static_for<I + 1, N>(f); }
}
constexpr int pv_done(int i) {
  if (i < QK0 + 6) return 0;
  if (i >= 32) return 16;
  const int p = pv_done(i - 1), a = i - 16 - PVLAG + 1;
  return a < p ? p : (a < p + PVMAX ? a : p + PVMAX);
}
constexpr int SL16(int n16) { return (n16 * DLT) >> 8; }
constexpr int GSL = GSLV;
constexpr int tail_pos(int i) { return 3 * (i < 6 ? 16 * i : i < 9 ? 96 + 4 * (i - 6) : 108 + 16 * (i - 9)); }
constexpr int kv_pos(int i) {
  const int g = i - G0 < 0 ? 0 : (i - G0 > 16 ? 16 : i - G0);
  return 128 * i + GSL * g + (i >= VGAP ? tail_pos(TPRE) : 0);
}
constexpr int PB1 = kv_pos(SB) - (SB == VGAP ? tail_pos(TPRE) : 0), PB2 = kv_pos(32);
constexpr int TT(int p) { return (p < PB1 ? p * DLT : p < PB2 ? PB1 * DLT + (p - PB1) * DLT2 : PB1 * DLT + (PB2 - PB1) * DLT2 + (p - PB2) * DLT3) >> 8; }
constexpr int recent(int i) {
  int n = 0;
  for (int j = 0; j < i; ++j) n += TT(kv_pos(i)) - (j ? TT(kv_pos(j)) : -KV0) < LT;
  return n;
}
template <int N> __device__ __forceinline__ void fall_back(u32& tb) {
  u32 d;
  if constexpr (BM == 0)
    asm volatile("s_getreg_b32 %1, hwreg(7, 0, 4)\n\ts_cmp_gt_u32 %1, %2\n\ts_cselect_b32 %1, %3, 0\n\ts_add_u32 %0, %0, %1"
                 : "+s"(tb), "=&s"(d) : "i"(N), "i"(BACK) : "scc");
  else if constexpr (BM == 1)
    asm volatile("s_getreg_b32 %1, hwreg(7, 0, 4)\n\ts_sub_i32 %1, %1, %2\n\ts_max_i32 %1, %1, 0\n\ts_mul_i32 %1, %1, %3\n\ts_add_u32 %0, %0, %1"
                 : "+s"(tb), "=&s"(d) : "i"(N), "i"(BACK) : "scc");
  else
    asm volatile("s_getreg_b32 %1, hwreg(7, 0, 4)\n\ts_sub_i32 %1, %1, %2\n\ts_max_i32 %1, %1, 0\n\ts_mul_i32 %1, %1, %1\n\ts_mul_i32 %1, %1, %3\n\ts_add_u32 %0, %0, %1"
                 : "+s"(tb), "=&s"(d) : "i"(N), "i"(BACK) : "scc");
}
constexpr int AUX_SC1 = 16, AUX_NT = 2;
__device__ __forceinline__ rsrc_t mkrs(const void* p, int bytes) { return __builtin_amdgcn_make_buffer_rsrc((void*)p, 0, bytes, 0x00020000); }
__device__ __forceinline__ v4u ld_nt(rsrc_t r, int voff, int soff) { return __builtin_amdgcn_raw_buffer_load_b128(r, voff, soff, AUX_NT); }
__device__ __forceinline__ v4u ld_sc1(rsrc_t rs, int off) { return __builtin_amdgcn_raw_buffer_load_b128(rs, off, 0, AUX_SC1); }
__device__ __forceinline__ void st_pair(rsrc_t rs, int off, u32 a, u32 b, u32 T0, u32 T1) {
  v4u v = {a, T0, b, T1};
  __builtin_amdgcn_raw_buffer_store_b128(v, rs, off, 0, AUX_SC1);
}
__device__ __forceinline__ bool tagged(v4u v, u32 T0, u32 T1) { return v[1] == T0 && v[3] == T1; }
typedef u32 v8u __attribute__((ext_vector_type(8)));
#define GV(p, k, j) "v[48+8*" #p "+4*" #k "+" #j "]"
#define GLD(p, k) "buffer_load_dwordx4 v[48+8*" #p "+4*" #k ":48+8*" #p "+4*" #k "+3], %[o" #k "], %[r], 0 offen sc1\n\t"
#define GML(p, k) "s_orn2_b64 exec, 1, %[m" #k "]\n\t" GLD(p, k)
#define GCK(p, k) "v_cmp_eq_u32 vcc, %[t0], " GV(p, k, 1) "\n\tv_cmp_eq_u32 %[x], %[t1], " GV(p, k, 3) "\n\ts_and_b64 vcc, vcc, %[x]\n\t" \
                  "v_cndmask_b32 %[a" #k "], %[a" #k "], " GV(p, k, 0) ", vcc\n\tv_cndmask_b32 %[b" #k "], %[b" #k "], " GV(p, k, 2) ", vcc\n\t" \
                  "s_or_b64 %[m" #k "], %[m" #k "], vcc\n\t"
#define GST(p) "s_waitcnt vmcnt(%[w])\n\t" GCK(p, 0) GCK(p, 1) "s_and_b64 %[x], %[m0], %[m1]\n\ts_cmp_eq_u64 %[x], -1\n\ts_cbranch_scc1 .Lge%=\n\t" \
               GML(p, 0) GML(p, 1) "s_mov_b64 exec, -1\n\t"
#define GEND "s_sub_u32 %[n], %[n], 1\n\ts_cbranch_scc0 .Lgl%=\n.Lge%=:"
struct Polls { v8u g0, g1, g2, g3; };
template <int R, int SP> __device__ __forceinline__ void gather(float (&pa)[2], float (&pb)[2], Polls& g, v4u rs, int off0, int off1, u64 m1, u32 tp, u32 T0, u32 T1) {
  static_assert(R >= 1 && R <= 4, "");
#define G1ST(p, reg, var) \
  if constexpr (p < R) { \
    wait_until(tp + p * SP); \
    asm volatile(GLD(p, 0) GLD(p, 1) : "=&{" reg "}"(var) : [o0] "v"(off0), [o1] "v"(off1), [r] "s"(rs) : "memory"); \
  } else asm volatile("" : "={" reg "}"(var));
  G1ST(0, "v[48:55]", g.g0) G1ST(1, "v[56:63]", g.g1) G1ST(2, "v[64:71]", g.g2) G1ST(3, "v[72:79]", g.g3)
  u64 m0 = 0, x;
  u32 cap = SPIN_CAP;
#define GRUN(text) \
  asm volatile(".Lgl%=:\n\t" text GEND \
               : "+{v[48:55]}"(g.g0), "+{v[56:63]}"(g.g1), "+{v[64:71]}"(g.g2), "+{v[72:79]}"(g.g3), [a0] "=&v"(pa[0]), [b0] "=&v"(pb[0]), [a1] "=&v"(pa[1]), \
                 [b1] "=&v"(pb[1]), [m0] "+s"(m0), [m1] "+s"(m1), [x] "=&s"(x), [n] "+s"(cap) \
               : [o0] "v"(off0), [o1] "v"(off1), [r] "s"(rs), [t0] "s"(T0), [t1] "s"(T1), [w] "i"(2 * (R - 1)) : "memory", "scc", "vcc")
  if constexpr (R == 1) GRUN(GST(0));
  if constexpr (R == 2) GRUN(GST(0) GST(1));
  if constexpr (R == 3) GRUN(GST(0) GST(1) GST(2));
  if constexpr (R == 4) GRUN(GST(0) GST(1) GST(2) GST(3));
}
__device__ __forceinline__ void keep(Polls& g) {
  asm volatile("s_waitcnt vmcnt(0)" ::"{v[48:55]}"(g.g0), "{v[56:63]}"(g.g1), "{v[64:71]}"(g.g2), "{v[72:79]}"(g.g3) : "memory");
}
#define STAMP(k) do { } while (0)
#define FLUSH() do { } while (0)
#define TRC(i) do { } while (0)
#define TRT(i) do { } while (0)
#define TRFL() do { } while (0)
#define TRW(k) do { } while (0)

typedef const __attribute__((address_space(4))) u64* kar_t;
__device__ __forceinline__ u64 late_arg(int i) {
  kar_t ka = (kar_t)__builtin_amdgcn_kernarg_segment_ptr();
  asm volatile("" : "+s"(ka));
  return ka[i];
}
__global__ void __launch_bounds__(NTHR, 3) __attribute__((aligned(4096)))
fused_kernel(const u16* __restrict__ kc_, const u16* __restrict__ wq_, const u16* __restrict__ x_, unsigned char* ws_,
             const u16* __restrict__ vc_, const u16* __restrict__ wg_, const u16* __restrict__ wu_, const u16* wo_a,
             const u16* wd_a, u16* out_a) {
  __shared__ __attribute__((aligned(16))) u32 lds_raw[L_WORDS];
  LAS u32* L = (LAS u32*)lds_raw;
  const int b = blockIdx.x;
  const int tid = threadIdx.x;
  const int h = (b & 7) * 2 + ((b >> 3) & 1), s = b >> 4;
  v4u W0[6], xa, xb;
  v2u ep;
  u32 cpf;
  constexpr int KST = 8192 << KVP, KLEN = 15 * KST + 8192;
  const u32 kvb = h * (SK * DH * 2) + (s >> KVP) * (16 * KST) + (s & ((1 << KVP) - 1)) * 8192;
  const int ko = ((tid & 0x3c3) | ((tid & 12) << 2) | ((tid & 48) >> 2)) * 16;
  {
    u64 pc; u32 len;
    asm volatile("s_getpc_b64 %0\n.Lfk_pc:\n\ts_mov_b32 %1, .Lfk_e0-.Lfk_pc\n\ts_max_i32 %1, %1, .Lfk_e1-.Lfk_pc\n\ts_max_i32 %1, %1, .Lfk_e2-.Lfk_pc"
                 : "=s"(pc), "=s"(len) : : "scc");
    cpf = __builtin_amdgcn_raw_buffer_load_b32(__builtin_amdgcn_make_buffer_rsrc((void*)pc, 0, len, 0x00020000), tid * 64, 0, 0);
  }
  asm volatile("" ::"s"(kc_), "s"(wq_), "s"(x_), "s"(ws_), "s"(vc_), "s"(wg_), "s"(wu_), "v"(ko), "s"(kvb));
  const u32 rot = (ROT == 0 ? 0 : ROT == 1 ? s : ROT == 2 ? h : ROT == 3 ? (s + h) & 15 : ROT == 4 ? (b & 15) : ROT == 6 ? (h * 5) & 15 : ROT == 7 ? 15 - h : ((s * 5 + h * 3) & 15)) * KST;
  const v4u K0 = ld_nt(mkrs((const char*)kc_ + (kvb + rot), 8192), ko, 0);
  SCHEDB();
  u64 t64 = __builtin_amdgcn_s_memrealtime();
  const int wave = __builtin_amdgcn_readfirstlane(tid >> 6);
  const int lane = tid & 63;
  const int cw = wave - 8;
  const int r0 = 144 * (s & 3) + 48 * (cw - 1);
  if (wave >= 8) {
    const rsrc_t rq = mkrs(wq_, wave == 8 ? 0 : D * QD * 2), rx = mkrs(x_, wave == 8 ? D * 2 : 0), re = mkrs(ws_, wave == 8 ? 8 : 0);
    const int qo = (r0 + (lane >> 3)) * (QD * 2) + (256 * h + 64 * (s >> 2)) * 2 + (lane & 7) * 16;
    xa = __builtin_amdgcn_raw_buffer_load_b128(rx, lane * 16, 0, 0);
    xb = __builtin_amdgcn_raw_buffer_load_b128(rx, 1024 + (lane & 7) * 16, 0, 0);
    ep = __builtin_amdgcn_raw_buffer_load_b64(re, WS_EPOCH, 0, AUX_SC1);
    SCHEDB();
#pragma unroll
    for (int i = 0; i < 6; ++i) W0[i] = ld_nt(rq, qo, i * 8 * QD * 2);
  }
  SCHEDB();
  asm volatile("" : "+s"(t64));
  const u32 t0 = (u32)t64 + (wave >= 8 ? TOFF : 0);
  const rsrc_t rk = mkrs((const char*)kc_ + kvb, KLEN);
  const rsrc_t rs = mkrs(ws_, WS_END);
  const v4u rsv = {(u32)(uintptr_t)ws_, (u32)((uintptr_t)ws_ >> 32) & 0xffffu, (u32)WS_END, 0x00020000u};
  const rsrc_t rv = mkrs((const char*)vc_ + kvb, KLEN);
  if (wave == 8) {
    L[lane] = (lane >> 4) == 2 ? 1u : 0u;
    LGKM0();
  }
  v4u mk;
  {
    const int e = lane & 7;
    const u32 hv = (e & 1) ? 0xffff0000u : 0x0000ffffu;
    mk[0] = (e >> 1) == 0 ? hv : 0u; mk[1] = (e >> 1) == 1 ? hv : 0u; mk[2] = (e >> 1) == 2 ? hv : 0u; mk[3] = (e >> 1) == 3 ? hv : 0u;
  }
  const int par = (lane >> 3) & 1;
  LAS u32* xt = L + L_XT;
  __builtin_amdgcn_s_barrier();
  u32 T0 = 0, T1 = 0;

  if (wave == 8) {
    __builtin_amdgcn_s_setprio(CPRIO);
    float v[16];
#pragma unroll
    for (int k = 0; k < 4; ++k) { v[2 * k] = bf_lo(xa[k]); v[2 * k + 1] = bf_hi(xa[k]); v[8 + 2 * k] = bf_lo(xb[k]); v[9 + 2 * k] = bf_hi(xb[k]); }
    const bool tail = lane < 8;
    float s0 = ((v[0] + v[1]) + (v[2] + v[3])) + ((v[4] + v[5]) + (v[6] + v[7]));
    float s1 = ((v[8] + v[9]) + (v[10] + v[11])) + ((v[12] + v[13]) + (v[14] + v[15]));
    const float mean = xrow_sum(row_sum(s0 + (tail ? s1 : 0.f))) * (1.f / D);
    float q0 = 0.f, q1 = 0.f;
#pragma unroll
    for (int k = 0; k < 8; ++k) { float d0 = v[k] - mean, d1 = v[8 + k] - mean; q0 += d0 * d0; q1 += d1 * d1; }
    const float var = xrow_sum(row_sum(q0 + (tail ? q1 : 0.f))) * (1.f / D);
    const float mb = rbf(mean), vb = rbf(var);
    const float rden = rcp(rbf(__builtin_amdgcn_sqrtf(rbf(vb + 1e-5f))));
#pragma unroll
    for (int k = 0; k < 8; ++k) {
      const float a = rbf(v[k] - mb) * rden;
      xt[k * 80 + lane] = cvtpk(a, a);
    }
    if (tail) {
#pragma unroll
      for (int k = 0; k < 8; ++k) {
        const float a = rbf(v[8 + k] - mb) * rden;
        xt[k * 80 + 64 + lane] = cvtpk(a, a);
      }
    }
    const u64 e1 = (((u64)(u32)__builtin_amdgcn_readfirstlane(ep[1]) << 32) | (u32)__builtin_amdgcn_readfirstlane(ep[0])) + 1;
    T0 = (u32)e1;
    T1 = (u32)(e1 >> 32) + T0 * 0x9E3779B1u;
    if (lane == 0) *(LAS v2u*)(L + L_TAG) = v2u{T0, T1};
    lds_signal(L + L_FLAG + F_XN, lane);
    __builtin_amdgcn_s_setprio(0);
    STAMP(14);
  } else if (wave > 8) {
    __builtin_amdgcn_s_setprio(CPRIO);
    lds_wait_ge(L + L_FLAG + F_XN, 1);
    const LAS u32* xr = xt + (lane >> 3) * 80 + (r0 >> 3);
    const v2u tg = *(const LAS v2u*)(L + L_TAG);
    f32x4 a0 = {0.f, 0.f, 0.f, 0.f}, a1 = {0.f, 0.f, 0.f, 0.f};
#pragma unroll
    for (int i = 0; i < 6; i += 2) {
      const v2u xv = *(const LAS v2u*)(xr + i);
      a0 = mfma(xv[0] & mk, W0[i], a0);
      a1 = mfma(xv[1] & mk, W0[i + 1], a1);
    }
    a0 += a1;
    T0 = __builtin_amdgcn_readfirstlane(tg[0]);
    T1 = __builtin_amdgcn_readfirstlane(tg[1]);
    if ((lane >> 5) == par) {
      LAS u32* g = L + L_QP + (8 * (lane & 7) + 4 * ((lane >> 4) & 1)) * 8 + (cw - 1) * 2 + par;
#pragma unroll
      for (int r = 0; r < 4; ++r) g[r * 8] = f2u(a0[r]);
    }
    const u32 old = lds_arrive(L + L_FLAG + F_QP, lane);
    if (old == 2) {
      const LAS u32* qp = L + L_QP + (lane & 31) * 16;
      const f32x4 a = *(const LAS f32x4*)qp, a2 = *(const LAS f32x4*)(qp + 8);
      const f32x2 c = *(const LAS f32x2*)(qp + 4), c2 = *(const LAS f32x2*)(qp + 12);
      const float q = ((a[0] + a[1]) + (a[2] + a[3])) + (c[0] + c[1]);
      const float qn = ((a2[0] + a2[1]) + (a2[2] + a2[3])) + (c2[0] + c2[1]);
      if (lane < 32) st_pair(rs, WS_Q + ((h * 4 + (s & 3)) * 128 + 32 * (s >> 2) + lane) * 16, f2u(q), f2u(qn), T0, T1);
      STAMP(5);
    }
  }

  if (wave < 8) {
    f32x4 acc0 = {0.f, 0.f, 0.f, 0.f};
    int rec = WS_REC + (((h * 16 + ((lane & 15) >> 1)) * 16 + s) * 8 + 4 * (lane & 1) + 2 * ((lane >> 4) & 1) + (lane >> 5)) * 16, rec2 = rec + 8 * 2048;
    asm volatile("" : "+v"(rec), "+v"(rec2));
    {
      v4u K[16], V[16];
      K[0] = K0;
      const int vo = tid * 16;
      const u32 tw0 = KV0 + ((wave * DLT) >> 4);
      u32 tb = t0 + tw0;
      const int hb = par;
      const bool t1 = (lane >> 2) & 1;
      v4u Be, Bo;
      float sc[8];
      u32 pk[8];
      float m = -INFINITY, ls = 0.f;
      f32x4 acc1 = {0.f, 0.f, 0.f, 0.f};
      __builtin_amdgcn_s_setprio(APRIO);
      static_for<1, 31>([&](auto ic) __attribute__((always_inline)) {
        constexpr int i = decltype(ic)::value;
        if constexpr (i < 32) {
          wait_until(tb + TT(kv_pos(i)));
          TRC(i);
          if constexpr (BACK && i >= B0 && i < 31) {
            fall_back<recent(i)>(tb);
          }
          if constexpr (i > WIN) vmwait<WIN>();
          if constexpr (i == 1) V[0] = ld_nt(rv, vo, rot);
          else if constexpr (i < 17) K[i - 1] = ld_nt(rk, ko, (rot + (i - 1) * KST) & (16 * KST - 1));
          else V[i - 16] = ld_nt(rv, vo, (rot + (i - 16) * KST) & (16 * KST - 1));
          SCHEDB();
          TRT(i);
          SCHEDB();
        }
        if constexpr (i == WIN + 1) asm volatile("" ::"v"(cpf));
        if constexpr (i == QK0) {
          lds_wait_ge(L + L_FLAG + F_QRDY, 1);
          if (wave == 0) STAMP(2);
          const v4u qf = *(const LAS v4u*)(L + L_Q + hb * 64 + (lane >> 4) * 16 + (lane & 3) * 4);
          const v4u zero4 = {0u, 0u, 0u, 0u};
          Be = t1 ? zero4 : qf; Bo = t1 ? qf : zero4;
        }
        if constexpr (i >= QK0 && i < QK0 + 4) {
#pragma unroll
          for (int j = 2 * (i - QK0); j < 2 * (i - QK0) + 2; ++j) {
            f32x4 d = {0.f, 0.f, 0.f, 0.f};
            d = mfma(K[2 * j], Be, d);
            d = mfma(K[2 * j + 1], Bo, d);
            float v = (dpp<0x00>(d[0]) + dpp<0x55>(d[1])) + (dpp<0xAA>(d[2]) + dpp<0xFF>(d[3]));
            v = rbf(rbf(v) * 0.08838834764831845f);
            sc[j] = v;
            m = fmaxf(m, v);
          }
        }
        if constexpr (i == QK0 + 3) {
          m = fmaxf(m, dpp<HMIR>(m));
          m = xrow_max(m);
          if ((lane & 55) == 0) L[L_MX + hb * 8 + wave] = f2u(m);
          lds_signal(L + L_FLAG + F_MAXC, lane);
        }
        if constexpr (i == QK0 + 5) {
          lds_wait_ge(L + L_FLAG + F_MAXC, 8);
          const f32x4 a = *(const LAS f32x4*)(L + L_MX + hb * 8), c = *(const LAS f32x4*)(L + L_MX + hb * 8 + 4);
          m = fmaxf(fmaxf(fmaxf(a[0], a[1]), fmaxf(a[2], a[3])), fmaxf(fmaxf(c[0], c[1]), fmaxf(c[2], c[3])));
        }
        if constexpr (i == QK0 + 5 || i == QK0 + 6) {
#pragma unroll
          for (int j = (i == QK0 + 5 ? 0 : XS); j < (i == QK0 + 5 ? XS : 8); ++j) {
            const float p = __expf(sc[j] - m);
            ls += p;
            pk[j] = cvtpk(p, p);
          }
        }
        if constexpr (i == QK0 + 6) {
          ls += dpp<HMIR>(ls);
          ls = xrow_sum(ls);
          if ((lane & 55) == 0) L[L_LS + hb * 8 + wave] = f2u(ls);
          L[L_PK + tid] = pk[7];
          lds_signal(L + L_FLAG + F_LS, lane);
          if (wave == 0) STAMP(3);
        }
        if constexpr (i == QK0 + 4) {
          const v2u tg = *(const LAS v2u*)(L + L_TAG);
          T0 = __builtin_amdgcn_readfirstlane(tg[0]); T1 = __builtin_amdgcn_readfirstlane(tg[1]);
        }
        if constexpr (i >= QK0 + 6) {
          constexpr int lo = pv_done(i - 1), hi = pv_done(i);
#pragma unroll
          for (int j = lo; j < hi; ++j) {
            const u32 pp = pk[j >> 1], pm = dppu<HMIR>(pp);
            if (j & 1) acc1 = mfma(v4u{pm & mk[0], pm & mk[1], pp & mk[2], pp & mk[3]}, V[j], acc1);
            else acc0 = mfma(v4u{pp & mk[0], pp & mk[1], pm & mk[2], pm & mk[3]}, V[j], acc0);
          }
        }
        SCHEDB();
      });
      auto rest = [&]() __attribute__((always_inline)) {
#pragma unroll
        for (int j = IC<pv_done(30)>::value; j < 15; ++j) {
          const u32 pp = pk[j >> 1], pm = dppu<HMIR>(pp);
          if (j & 1) acc1 = mfma(v4u{pm & mk[0], pm & mk[1], pp & mk[2], pp & mk[3]}, V[j], acc1);
          else acc0 = mfma(v4u{pp & mk[0], pp & mk[1], pm & mk[2], pm & mk[3]}, V[j], acc0);
        }
      };
      if (wave) {
        rest();
        *(LAS f32x4*)(L + L_ORED + tid * 4) = acc0 + acc1;
        lds_signal(L + L_FLAG + F_DONE, lane);
        if (wave == 7) STAMP(15);
      } else {
        v4u VF[8];
#pragma unroll
        for (int w = 0; w < 8; ++w) {
          if (w % FW == 0) wait_until(tb + TT(kv_pos(31)) + (((2 * w + FO) * DLT) >> 5));
          if (w == 0) vmwait<WIN>();
          VF[w] = ld_nt(rv, vo, ((rot + 15 * KST) & (16 * KST - 1)) + w * 1024);
          SCHEDB();
          if ((GATE == 1 || GATE == 2) && w == (GATE == 1 ? 7 : 3)) lds_signal(L + L_FLAG + F_VI, lane);
        }
        STAMP(1);
        const volatile LAS v4u* fp = (const volatile LAS v4u*)(L + L_SC);
        v4u fs;
#pragma unroll 1
        for (u32 i = 0; i < (1u << 20); ++i) {
          fs = *fp;
          if (__builtin_amdgcn_readfirstlane(fs[2])) break;
          __builtin_amdgcn_s_sleep(1);
        }
        asm volatile("" ::: "memory");
        v4u AF[8];
#pragma unroll
        for (int w = 0; w < 8; ++w) {
          const u32 pp = w ? L[L_PK + w * 64 + lane] : pk[7], pm = dppu<HMIR>(pp);
          AF[w] = v4u{pm & mk[0], pm & mk[1], pp & mk[2], pp & mk[3]};
          asm volatile("" : "+v"(AF[w]));
        }
        SCHEDB();
        if (GATE == 3) { vmwait<9>(); lds_signal(L + L_FLAG + F_VI, lane); }
        rest();
        if (GATE == 4) { vmwait<8>(); lds_signal(L + L_FLAG + F_VI, lane); }
        SCHEDB();
        lds_wait_ge(L + L_FLAG + F_DONE, 7);
        STAMP(16);
        f32x4 r[7];
#pragma unroll
        for (int w = 0; w < 7; ++w) r[w] = *(const LAS f32x4*)(L + L_ORED + (w + 1) * 256 + lane * 4);
        SCHEDB();
#pragma unroll
        for (int w = 0; w < 6; w += 2) {
          acc0 = mfma(AF[w], VF[w], acc0);
          acc1 = mfma(AF[w + 1], VF[w + 1], acc1);
        }
        SCHEDB();
        f32x4 t = ((r[0] + r[1]) + (r[2] + r[3])) + ((r[4] + r[5]) + r[6]);
        t += acc0 + acc1;
        SCHEDB();
        t = mfma(AF[6], VF[6], t);
        SCHEDB();
        t = mfma(AF[7], VF[7], t);
        auto s0 = __builtin_amdgcn_permlane32_swap(f2u(t[0]), f2u(t[2]), false, false);
        auto s1 = __builtin_amdgcn_permlane32_swap(f2u(t[1]), f2u(t[3]), false, false);
        const float f0 = u2f(fs[0]), f1 = u2f(fs[1]);
        st_pair(rs, rec, f2u(u2f(s0[0]) * f0), f2u(u2f(s1[0]) * f0), T0, T1);
        st_pair(rs, rec2, f2u(u2f(s0[1]) * f1), f2u(u2f(s1[1]) * f1), T0, T1);
        lds_signal(L + L_FLAG + F_DONE, lane);
        STAMP(4);
      }
    }
    TRT(32);
    if (b >= RED0) {
      const int r = b - RED0, rr = r / (8 / PP), fl = lane & (2 * PP - 1);
      const int col = 64 * (rr >> 2) + 4 * ((rr >> 1) & 1) + 2 * (rr & 1) + 8 * ((r % (8 / PP)) * PP + (fl >> 1)) + (lane & 1);
      u32 xo = x_[col];
      const rsrc_t rout = mkrs((void*)late_arg(9), D * 2);
      const int pb = WS_PART + (r * NWG + wave * 32) * (PP * 16) + lane * 16;
      Polls gp;
      float ga[2], gb[2];
      lds_wait_ge(L + L_FLAG + F_PUB, 3);
      asm volatile("" : "+v"(xo));
      if (wave == 0) STAMP(12);
      gather<NP, PSP>(ga, gb, gp, rsv, pb, pb + 1024, 0, now() + PDEL, T0, T1);
      if (wave == 0) STAMP(23);
      float a0 = ga[0] + ga[1], a1 = gb[0] + gb[1];
      a0 += dpp<XOR8>(a0); a1 += dpp<XOR8>(a1);
      a0 += dpp<0x124>(a0); a1 += dpp<0x124>(a1);
      a0 = xrow_sum(a0); a1 = xrow_sum(a1);
      if (lane < PP) *(LAS v2u*)(L + L_FIN + wave * 16 + 2 * lane) = v2u{f2u(a0), f2u(a1)};
      u32 old2 = lds_add_nw(L + L_FLAG + F_FIN);
      const LAS float* f = (const LAS float*)(L + L_FIN) + fl;
      const float A = ((f[0] + f[16]) + (f[32] + f[48])) + ((f[64] + f[80]) + (f[96] + f[112]));
      float o = bf_lo(xo) + A;
      asm volatile("" : "+v"(old2), "+v"(o));
      old2 = __builtin_amdgcn_readfirstlane(old2);
      if (old2 == 7) {
        __builtin_amdgcn_raw_buffer_store_b16((u16)cvtpk(o, o), rout, lane < 2 * PP ? col * 2 : OOB, 0, 0);
        const v2u e1 = {T0, T1 - T0 * 0x9E3779B1u};
        __builtin_amdgcn_raw_buffer_store_b64(e1, rs, r == 0 && lane == 0 ? WS_EPOCH : OOB, 0, AUX_SC1);
        STAMP(13);
        asm volatile(".Lfk_e0:");
      }
      keep(gp);
    }
    FLUSH();
    TRFL();
    return;
  }

  asm volatile("" ::"v"(cpf), "v"(K0));
  const int c = cw - 1;
  if (wave > 8) {
    v4u Wo[6];
    f32x4 acc[3];
    const int ct0 = 3 * c;
    {
      const int gpi = 8 * s + (b & 7);
      if (GP ? gpi < NGU / 2 : b < NGU) {
        const int gcg = GP ? 2 * (gpi / GKS) + ((b >> 3) & 1) : b / GKS, gks = (GP ? gpi : b) % GKS;
        const int grp0 = 24 * gks + 8 * c;
        const rsrc_t rg = mkrs(wg_, D * FF * 2), ru = mkrs(wu_, D * FF * 2);
        const int go = (grp0 * 8 + (lane >> 3)) * (FF * 2) + gcg * 128 + (lane & 7) * 16;
        u32 tg = t0 + KV0 + SL16(128 * G0) + c * SL16((128 + GSL) / 3);
        v4u Wg[16];
#pragma unroll
        for (int i = 0; i < 16; ++i) {
          wait_until(tg + SL16((128 + GSL) * i));
          TRC(1 + i); TRT(1 + i);
          if (GB && BACK && i >= 2 && i < 15) fall_back<(LT - 1) / SL16(128 + GSL)>(tg);
          Wg[i] = ld_nt((i & 1) ? ru : rg, go, (i >> 1) * (8 * FF * 2));
          SCHEDB();
        }
        TRW(17);
        const LAS u32* xr = xt + (lane >> 3) * 80 + grp0;
        f32x4 ag = {0.f, 0.f, 0.f, 0.f}, au = {0.f, 0.f, 0.f, 0.f};
#pragma unroll
        for (int i = 0; i < 8; ++i) {
          const v4u A = xr[i] & mk;
          ag = mfma(A, Wg[2 * i], ag);
          au = mfma(A, Wg[2 * i + 1], au);
        }
        const f32x2 fg = fold<0>(ag), fu = fold<1>(au);
        const int cc = 8 * (lane & 7) + 4 * ((lane >> 4) & 1) + 2 * (lane >> 5);
        LAS u32* g = L + L_GU + (par * 64 + cc) * 4 + c;
        g[0] = f2u(par ? fu[0] : fg[0]);
        g[4] = f2u(par ? fu[1] : fg[1]);
        const u32 old = lds_arrive(L + L_FLAG + F_GU, lane);
        if (old == 2) {
          const f32x4 a = *(const LAS f32x4*)(L + L_GU + lane * 4), c4 = *(const LAS f32x4*)(L + L_GU + 256 + lane * 4);
          st_pair(rs, WS_H + (gks * FF + gcg * 64 + lane) * 16, f2u((a[0] + a[1]) + a[2]), f2u((c4[0] + c4[1]) + c4[2]), T0, T1);
          STAMP(8);
        }
      }
      const rsrc_t rd = mkrs((void*)late_arg(8), FF * D * 2), ro = mkrs((void*)late_arg(7), QD * D * 2);
      const int dof = (18 * b + (lane >> 3)) * (D * 2) + ct0 * 128 + (lane & 7) * 16;
      const int dof2 = lane < 16 ? dof : OOB;
      const int oof = (256 * h + 16 * s + (lane >> 3)) * (D * 2) + ct0 * 128 + (lane & 7) * 16;
      v4u Wd[9], Ah[3];
      u32 tt = t0 + KV0 + (TB ? 0 : c * SL16(16));
      if (GATE) {
        const u32 tg_ = t0 + KV0 + TT(kv_pos(32)) + TD;
        wait_long(tg_ - 8);
        lds_wait_ge(L + L_FLAG + F_VI, 1);
        const int late = (int)(now() - tg_);
        tt += late > 0 ? late : 0;
      }
#pragma unroll
      for (int i = 0; i < 15; ++i) {
        const u32 tw_ = tt + TT((i < TPRE ? kv_pos(VGAP) : kv_pos(32)) - tail_pos(TPRE) + tail_pos(i)) + (i < TPRE ? 0 : TD);
        if (i == 0 || i == TPRE) wait_long(tw_); else if (!TB || i % 3 == 0) wait_until(tw_);
        if (i == TPRE && cw == 1) STAMP(7);
        TRC(32 + i); TRT(32 + i);
        if (i < 9) Wd[i] = ld_nt(rd, i < 6 ? dof : dof2, (i / 3) * 8 * D * 2 + (i % 3) * 128);
        else Wo[i - 9] = ld_nt(ro, oof, ((i - 9) / 3) * 8 * D * 2 + (i % 3) * 128);
        SCHEDB();
      }
      TRW(47);
      lds_wait_ge(L + L_FLAG + F_H, 1);
#pragma unroll
      for (int j = 0; j < 3; ++j) Ah[j] = L[L_HV + 8 * j + (lane >> 3)] & mk;
#pragma unroll
      for (int t = 0; t < 3; ++t) {
        acc[t] = mfma(Ah[0], Wd[t], f32x4{0.f, 0.f, 0.f, 0.f});
        acc[t] = mfma(Ah[1], Wd[3 + t], acc[t]);
        acc[t] = mfma(Ah[2], Wd[6 + t], acc[t]);
      }
      if (cw == 1) STAMP(17);
    }
    const int po = WS_PART + (((4 * ct0 + 2 * ((lane >> 4) & 1) + (lane >> 5)) * (8 / PP) + (lane & 7) / PP) * NWG + b) * (PP * 16) + (lane & (PP - 1)) * 16;
    const LAS u32* ov = L + L_OV + (lane >> 3);
    u32 o0, o1;
#pragma unroll 1
    for (u32 i = 0; i < (1u << 20); ++i) {
      o0 = lds_ld(ov); o1 = lds_ld(ov + 8);
      if (__all(o0 != 1u && o1 != 1u)) break;
    }
    const v4u A0 = o0 & mk, A1 = o1 & mk;
    if (cw == 1) STAMP(18);
#pragma unroll
    for (int t = 0; t < 3; ++t) acc[t] = mfma(A0, Wo[t], acc[t]);
#pragma unroll
    for (int t = 0; t < 3; ++t) acc[t] = mfma(A1, Wo[3 + t], acc[t]);
    const f32x2 f0 = fold<0>(acc[0]), f1 = fold<1>(acc[1]), f2 = fold<0>(acc[2]);
    st_pair(rs, po + par * (4 * NWG * 128), f2u(par ? f1[0] : f0[0]), f2u(par ? f1[1] : f0[1]), T0, T1);
    if (!par) st_pair(rs, po + 2 * (4 * NWG * 128), f2u(f2[0]), f2u(f2[1]), T0, T1);
    lds_signal(L + L_FLAG + F_PUB, lane);
    if (cw == 1) STAMP(11);
    asm volatile(".Lfk_e1:");
    FLUSH();
    TRFL();
    return;
  }

  {
    {
      STAMP(20);
      __builtin_amdgcn_s_setprio(CPRIO);
      u32 tch, pre[5] = {0u, 0u, 0u, 0u, 0u};
      {
        u64 a7 = late_arg(7), a8 = late_arg(8), a9 = late_arg(9);
        asm volatile("" : "+s"(a7), "+s"(a8), "+s"(a9));
        u64 p = (lane & 1) ? a8 + (18 * b) * (D * 2) : a7 + (256 * h + 16 * s) * (D * 2);
        if (lane & 2) p = a9;
        tch = __hip_atomic_load((const GAS u32*)p, __ATOMIC_RELAXED, __HIP_MEMORY_SCOPE_AGENT);
      }
      auto preread = [&]() __attribute__((always_inline)) {
        const int j = lane - 46, k = (j >= 6) + (j >= 12);
        int po = (PRE & 2) ? WS_REC + (h * 16 + s) * 2048 + lane * 64 : OOB;
        if (lane >= 32) po = (PRE & 1) && lane < 34 ? WS_ML + (h * 16 + s) * 128 + (lane & 1) * 64 : OOB;
        if (lane >= 46) po = (PRE & 1) ? WS_H + (k * FF + 18 * b) * 16 + (j - 6 * k) * 56 : OOB;
        pre[0] = __builtin_amdgcn_raw_buffer_load_b32(rs, po, 0, AUX_SC1);
        if ((PRE & 4) && b >= RED0) {
#pragma unroll
          for (int i = 0; i < 4; ++i) pre[1 + i] = __builtin_amdgcn_raw_buffer_load_b32(rs, WS_PART + (b - RED0) * (NWG * PP * 16) + lane * 64, i * 4096, AUX_SC1);
        }
      };
      if (PRE && TPR < TQ) { wait_long(t0 + TPR); preread(); }
      wait_long(t0 + TQ);
      asm volatile("" ::"v"(tch));
      v4u g[8];
      bool ok = false;
#pragma unroll 1
      for (u32 it = 0; it < SPIN_CAP; ++it) {
        if (!ok) {
#pragma unroll
          for (int k = 0; k < 8; ++k) g[k] = ld_sc1(rs, WS_Q + (512 * h + 64 * k + lane) * 16);
        }
        ok = true;
#pragma unroll
        for (int k = 0; k < 8; ++k) ok = ok && tagged(g[k], T0, T1);
        if (__all(ok)) break;
        __builtin_amdgcn_s_sleep(16);
        asm volatile("" ::: "memory");
      }
#pragma unroll
      for (int k = 0; k < 2; ++k) {
        const float qa = (u2f(g[k][0]) + u2f(g[2 + k][0])) + (u2f(g[4 + k][0]) + u2f(g[6 + k][0]));
        const float qb = (u2f(g[k][2]) + u2f(g[2 + k][2])) + (u2f(g[4 + k][2]) + u2f(g[6 + k][2]));
        L[L_Q + 64 * k + lane] = cvtpk(qa, qb);
      }
      lds_signal(L + L_FLAG + F_QRDY, lane);
      STAMP(6);
      if (PRE && TPR >= TQ) { wait_long(t0 + TPR); preread(); }
      wait_long(t0 + KV0 + TT(kv_pos(QK0 + 6)) - 60);
      lds_wait_ge(L + L_FLAG + F_LS, 8);
      asm volatile("" ::"v"(pre[0]), "v"(pre[1]), "v"(pre[2]), "v"(pre[3]), "v"(pre[4]));
      {
        const LAS u32* sp = L + L_MX + ((lane ^ (lane >> 1)) & 1) * 8;
        const f32x4 a = *(const LAS f32x4*)sp, c4 = *(const LAS f32x4*)(sp + 4), e = *(const LAS f32x4*)(sp + 16), f = *(const LAS f32x4*)(sp + 20);
        const float Mh = fmaxf(fmaxf(fmaxf(a[0], a[1]), fmaxf(a[2], a[3])), fmaxf(fmaxf(c4[0], c4[1]), fmaxf(c4[2], c4[3])));
        const float Lh = ((e[0] + e[1]) + (e[2] + e[3])) + ((f[0] + f[1]) + (f[2] + f[3]));
        if (lane < 8) st_pair(rs, WS_ML + (h * 16 + s) * 128 + lane * 16, f2u(Mh), f2u(Lh), T0, T1);
      }
      STAMP(21);
      const int mo = WS_ML + (h * 16 + (lane >> 2)) * 128 + (lane & 3) * 16;
      const bool act = lane < 18;
      const int hk = (lane >= 18) + (lane >= 36);
      const int ho = lane < 54 ? WS_H + (hk * (FF - 18) + 18 * b + lane) * 16 : OOB;
      Polls mp;
      float ma[2], mb[2];
      {
        const u32 tn = now() + TML, th = t0 + TH;
        const u32 tm = (int)(tn - th) > 0 ? tn : th;
        wait_sl<12>(tm - 45);
        gather<NM, MSP>(ma, mb, mp, rsv, mo, ho, ~0ull << 54, tm, T0, T1);
      }
      {
        float M = fmaxf(ma[0], dpp<HMIR>(ma[0]));
        M = xrow_max(fmaxf(M, dpp<XOR8>(M)));
        const float w = __expf(ma[0] - M);
        float lt = opq(w * mb[0]);
        lt += dpp<HMIR>(lt);
        lt += dpp<XOR8>(lt);
        const float fc = w * rcp(xrow_sum(lt));
        const u32 f0 = __builtin_amdgcn_readlane(f2u(fc), 4 * s), f1 = __builtin_amdgcn_readlane(f2u(fc), 4 * s + 1);
        if (lane == 0) *(volatile LAS v4u*)(L + L_SC) = v4u{f0, f1, 1u, 0u};
        STAMP(22);
      }
      const int l1 = (lane + 18) * 4, l2 = (lane + 36) * 4;
      const float gs = (ma[1] + u2f(__builtin_amdgcn_ds_bpermute(l1, f2u(ma[1])))) + u2f(__builtin_amdgcn_ds_bpermute(l2, f2u(ma[1])));
      const float us = (mb[1] + u2f(__builtin_amdgcn_ds_bpermute(l1, f2u(mb[1])))) + u2f(__builtin_amdgcn_ds_bpermute(l2, f2u(mb[1])));
      const float gb = rbf(gs), ub = rbf(us);
      float hv = rbf(gb * rcp(1.f + __expf(-gb))) * ub;
      if (!act) hv = 0.f;
      if (lane < 24) L[L_HV + lane] = cvtpk(hv, hv);
      lds_signal(L + L_FLAG + F_H, lane);
      STAMP(9);
      keep(mp);
      wait_long(t0 + KV0 + TT(kv_pos(32)) - 60);
    }
    lds_wait_ge(L + L_FLAG + F_DONE, 8);
    const int off = WS_REC + (h * 16 + s) * 2048 + lane * 16;
    Polls rp;
    float ra[2], rb[2];
    STAMP(19);
    gather<NR, RSP>(ra, rb, rp, rsv, off, off + 1024, 0, now() + RDEL, T0, T1);
    STAMP(10);
    float oa = ra[0] + ra[1], ob = rb[0] + rb[1];
    oa += dpp<XOR8>(oa); ob += dpp<XOR8>(ob);
    oa = xrow_sum(oa); ob = xrow_sum(ob);
    asm volatile(".Lfk_e2:" : "+v"(oa), "+v"(ob));
    if (lane < 8) *(volatile LAS v2u*)(L + L_OV + 2 * lane) = v2u{cvtpk(oa, oa), cvtpk(ob, ob)};
    keep(rp);
  }
  FLUSH();
}

extern "C" void kernel_launch(void* const* d_in, const int* in_sizes, int n_in, void* d_out, int out_size, void* d_ws, size_t ws_size,
                              hipStream_t stream) {
  fused_kernel<<<NWG, NTHR, 0, stream>>>((const u16*)d_in[1], (const u16*)d_in[3], (const u16*)d_in[0], (unsigned char*)d_ws,
                                         (const u16*)d_in[2], (const u16*)d_in[6], (const u16*)d_in[7], (const u16*)d_in[4],
                                         (const u16*)d_in[8], (u16*)d_out);
}
```
